```python
import torch

def _softmax(x, axis=-1):
    return torch.softmax(x, dim=axis)
def _gelu(x):
    return torch.nn.functional.gelu(x, approximate="tanh")
def _silu(x):
    return torch.nn.functional.silu(x)
def _sigmoid(x):
    return torch.sigmoid(x)
def _layernorm(x, axis=-1, eps=1e-5):
    m = x.mean(dim=axis, keepdim=True)
    v = x.var(dim=axis, keepdim=True, unbiased=False)
    return (x - m) / torch.sqrt(v + eps)
def _scatter_add(tgt, idx, src, axis=0):
    return tgt.clone().index_add_(axis, idx, src)
def _segment_sum(src, seg, n):
    out = torch.zeros((n,) + src.shape[1:], dtype=src.dtype)
    return out.index_add_(0, seg, src)
def _pad(x, pw):
    flat = tuple(v for pair in reversed(pw) for v in pair)
    return torch.nn.functional.pad(x, flat)

kernel_name = 'concat_heads__sliding_block_cached__sliding_block_cached__decode'

def setup_inputs():

    _w = (lambda a: (lambda t: t.to(torch.bfloat16) if t.is_floating_point() else t)(torch.as_tensor(a)))
    def _A_setup():
        B, Sq, D = 2, 1, 512
        Hq, Hkv, Sk, Dh = 32, 8, 8192, 128
        assert Hq % Hkv == 0, (Hq, Hkv)
        return {
            "x": _w((lambda *s: torch.randn(*s, dtype=torch.bfloat16) * 0.8)(B, Sq, D)),
            "k_cache": _w((lambda *s: torch.randn(*s, dtype=torch.bfloat16) * 0.8)(B, Hkv, Sk, Dh)),
            "v_cache": _w((lambda *s: torch.randn(*s, dtype=torch.bfloat16) * 0.05)(B, Hkv, Sk, Dh)),
            "wq": _w((lambda *s: torch.randn(*s, dtype=torch.bfloat16) * 0.8)(D, Hq * Dh) * (D ** -0.5)),
            "wo": _w((lambda *s: torch.randn(*s, dtype=torch.bfloat16) * 0.05)(Hq * Dh, D) * ((Hq * Dh) ** -0.5)),
        }
    _A_in = _A_setup()

    def _B_setup():
        B, Sq, D = 2, 1, 512
        Hq, Hkv, Sk, Dh = 32, 8, 8192, 128
        assert Hq % Hkv == 0, (Hq, Hkv)
        return {
            "x": _w((lambda *s: torch.randn(*s, dtype=torch.bfloat16) * 0.8)(B, Sq, D)),
            "k_cache": _w((lambda *s: torch.randn(*s, dtype=torch.bfloat16) * 0.8)(B, Hkv, Sk, Dh)),
            "v_cache": _w((lambda *s: torch.randn(*s, dtype=torch.bfloat16) * 0.05)(B, Hkv, Sk, Dh)),
            "wq": _w((lambda *s: torch.randn(*s, dtype=torch.bfloat16) * 0.8)(D, Hq * Dh) * (D ** -0.5)),
            "wo": _w((lambda *s: torch.randn(*s, dtype=torch.bfloat16) * 0.05)(Hq * Dh, D) * ((Hq * Dh) ** -0.5)),
        }
    _B_in = _B_setup()

    return {
        "A_x": _A_in["x"],
        "A_k_cache": _A_in["k_cache"],
        "A_v_cache": _A_in["v_cache"],
        "A_wq": _A_in["wq"],
        "A_wo": _A_in["wo"],
        "B_x": _B_in["x"],
        "B_k_cache": _B_in["k_cache"],
        "B_v_cache": _B_in["v_cache"],
        "B_wq": _B_in["wq"],
        "B_wo": _B_in["wo"],
    }

def reference(A_x, A_k_cache, A_v_cache, A_wq, A_wo, B_x, B_k_cache, B_v_cache, B_wq, B_wo):
    def _A_ref(x, k_cache, v_cache, wq, wo):
        B, Sq, D = x.shape
        Hq, Dh = 32, 128
        Hkv, Sk = k_cache.shape[1], k_cache.shape[2]
        W = min(4096, max(256, Sk // 4))
        rep = Hq // Hkv
        q = (lambda x, *d: x.permute(*d))((x @ wq).reshape(B, Sq, Hq, Dh), 0, 2, 1, 3)
        k = (lambda x, n, axis: torch.repeat_interleave(x, n, dim=axis))(k_cache[:, :, Sk - W:, :], rep, axis=1)
        v = (lambda x, n, axis: torch.repeat_interleave(x, n, dim=axis))(v_cache[:, :, Sk - W:, :], rep, axis=1)
        scores = torch.einsum("bhqd,bhkd->bhqk", q, k) * (Dh ** -0.5)
        p = _softmax(scores, axis=-1)
        o = torch.einsum("bhqk,bhkd->bhqd", p, v)
        return (lambda x, *d: x.permute(*d))(o, 0, 2, 1, 3).reshape(B, Sq, Hq * Dh) @ wo

    def _B_ref(x, k_cache, v_cache, wq, wo):
        B, Sq, D = x.shape
        Hq, Dh = 32, 128
        Hkv, Sk = k_cache.shape[1], k_cache.shape[2]
        W = min(4096, max(256, Sk // 4))
        rep = Hq // Hkv
        q = (lambda x, *d: x.permute(*d))((x @ wq).reshape(B, Sq, Hq, Dh), 0, 2, 1, 3)
        k = (lambda x, n, axis: torch.repeat_interleave(x, n, dim=axis))(k_cache[:, :, Sk - W:, :], rep, axis=1)
        v = (lambda x, n, axis: torch.repeat_interleave(x, n, dim=axis))(v_cache[:, :, Sk - W:, :], rep, axis=1)
        scores = torch.einsum("bhqd,bhkd->bhqk", q, k) * (Dh ** -0.5)
        p = _softmax(scores, axis=-1)
        o = torch.einsum("bhqk,bhkd->bhqd", p, v)
        return (lambda x, *d: x.permute(*d))(o, 0, 2, 1, 3).reshape(B, Sq, Hq * Dh) @ wo

    a = _A_ref(A_x, A_k_cache, A_v_cache, A_wq, A_wo)
    b = _B_ref(B_x, B_k_cache, B_v_cache, B_wq, B_wo)
    return torch.concatenate([a, b], axis=-1)
```

```cpp
#include <hip/hip_runtime.h>
#include <hip/hip_bf16.h>
#include <stdint.h>

typedef unsigned int u32;
typedef u32 u32x4 __attribute__((ext_vector_type(4)));
typedef float f32x4 __attribute__((ext_vector_type(4)));

constexpr int B = 2, D = 512, HQ = 32, HKV = 8, SK = 8192, DH = 128, W = 2048, G = 4;
constexpr int NSPLIT = 8;

__device__ __forceinline__ float bflo(u32 x) { return __builtin_bit_cast(float, x << 16); }
__device__ __forceinline__ float bfhi(u32 x) { return __builtin_bit_cast(float, x & 0xffff0000u); }
__device__ __forceinline__ float rbf(float f) { return __bfloat162float(__float2bfloat16(f)); }

struct Args {
  const uint16_t* x[2]; const uint16_t* k[2]; const uint16_t* v[2]; const uint16_t* wq[2]; const uint16_t* wo[2];
  uint16_t* out; float* q; float* po; float* pml;
};

__global__ void __launch_bounds__(256) k_q(Args a) {
  __shared__ float red[64][2][32];
  const int br = blockIdx.x >> 7, cb = blockIdx.x & 127;
  const int tid = threadIdx.x, r = tid >> 2, c = tid & 3;
  const uint16_t* wq = a.wq[br]; const uint16_t* x = a.x[br];
  float acc[2][8] = {};
  u32x4 w[8];
#pragma unroll
  for (int i = 0; i < 8; ++i) w[i] = *(const u32x4*)(wq + (size_t)(r + 64 * i) * 4096 + cb * 32 + c * 8);
#pragma unroll
  for (int i = 0; i < 8; ++i) {
    float x0 = bflo(x[r + 64 * i]), x1 = bflo(x[512 + r + 64 * i]);
#pragma unroll
    for (int j = 0; j < 4; ++j) {
      acc[0][2 * j] += x0 * bflo(w[i][j]); acc[0][2 * j + 1] += x0 * bfhi(w[i][j]);
      acc[1][2 * j] += x1 * bflo(w[i][j]); acc[1][2 * j + 1] += x1 * bfhi(w[i][j]);
    }
  }
#pragma unroll
  for (int j = 0; j < 8; ++j) { red[r][0][c * 8 + j] = acc[0][j]; red[r][1][c * 8 + j] = acc[1][j]; }
  __syncthreads();
  if (tid < 64) {
    int b = tid >> 5, col = tid & 31; float s = 0.f;
    for (int i = 0; i < 64; ++i) s += red[i][b][col];
    a.q[(br * 2 + b) * 4096 + cb * 32 + col] = rbf(s);
  }
}

__global__ void __launch_bounds__(256) k_attn(Args a) {
  __shared__ float qs[4][128];
  __shared__ float ps[4][256];
  __shared__ float red[16][4][128];
  __shared__ float wm[4][4], wl[4][4];
  const int blk = blockIdx.x, s = blk & 7, kvh = (blk >> 3) & 7, b = (blk >> 6) & 1, br = blk >> 7;
  const int tid = threadIdx.x;
  for (int i = tid; i < 512; i += 256) qs[i >> 7][i & 127] = a.q[(br * 2 + b) * 4096 + kvh * 512 + i];
  __syncthreads();
  const size_t base = ((size_t)(b * HKV + kvh) * SK + (SK - W) + s * 256) * DH;
  const uint16_t* kp = a.k[br] + base + (size_t)tid * DH;
  float sc[4] = {};
#pragma unroll 4
  for (int i = 0; i < 16; ++i) {
    u32x4 kk = *(const u32x4*)(kp + i * 8);
#pragma unroll
    for (int j = 0; j < 4; ++j)
#pragma unroll
      for (int g = 0; g < 4; ++g) sc[g] += qs[g][i * 8 + 2 * j] * bflo(kk[j]) + qs[g][i * 8 + 2 * j + 1] * bfhi(kk[j]);
  }
  float m[4];
#pragma unroll
  for (int g = 0; g < 4; ++g) { sc[g] *= 0.08838834764831845f; m[g] = sc[g];
    for (int o = 32; o; o >>= 1) m[g] = fmaxf(m[g], __shfl_xor(m[g], o));
    if ((tid & 63) == 0) wm[tid >> 6][g] = m[g]; }
  __syncthreads();
  float l[4];
#pragma unroll
  for (int g = 0; g < 4; ++g) { m[g] = fmaxf(fmaxf(wm[0][g], wm[1][g]), fmaxf(wm[2][g], wm[3][g]));
    float p = __expf(sc[g] - m[g]); ps[g][tid] = p; l[g] = p;
    for (int o = 32; o; o >>= 1) l[g] += __shfl_xor(l[g], o);
    if ((tid & 63) == 0) wl[tid >> 6][g] = l[g]; }
  __syncthreads();
  const int r = tid >> 4, c = tid & 15;
  float acc[4][8] = {};
  const uint16_t* vp = a.v[br] + base;
#pragma unroll 4
  for (int i = 0; i < 16; ++i) {
    u32x4 vv = *(const u32x4*)(vp + (size_t)(r + 16 * i) * DH + c * 8);
#pragma unroll
    for (int g = 0; g < 4; ++g) { float p = ps[g][r + 16 * i];
#pragma unroll
      for (int j = 0; j < 4; ++j) { acc[g][2 * j] += p * bflo(vv[j]); acc[g][2 * j + 1] += p * bfhi(vv[j]); } }
  }
#pragma unroll
  for (int g = 0; g < 4; ++g)
#pragma unroll
    for (int j = 0; j < 8; ++j) red[r][g][c * 8 + j] = acc[g][j];
  __syncthreads();
  for (int i = tid; i < 512; i += 256) { int g = i >> 7, d = i & 127; float sum = 0.f;
    for (int rr = 0; rr < 16; ++rr) sum += red[rr][g][d];
    a.po[((size_t)((br * 2 + b) * 32 + kvh * 4 + g) * NSPLIT + s) * 128 + d] = sum; }
  if (tid < 4) { int g = tid; float L = wl[0][g] + wl[1][g] + wl[2][g] + wl[3][g];
    float M = fmaxf(fmaxf(wm[0][g], wm[1][g]), fmaxf(wm[2][g], wm[3][g]));
    a.pml[(((br * 2 + b) * 32 + kvh * 4 + g) * NSPLIT + s) * 2] = M; a.pml[(((br * 2 + b) * 32 + kvh * 4 + g) * NSPLIT + s) * 2 + 1] = L; }
}

__global__ void __launch_bounds__(1024) k_o(Args a) {
  __shared__ float red[16][2][8];
  const int br = blockIdx.x >> 6, cb = blockIdx.x & 63, tid = threadIdx.x;
  float acc[2][8] = {};
#pragma unroll 1
  for (int i = 0; i < 4; ++i) {
    const int row = tid + 1024 * i, h = row >> 7, d = row & 127;
    u32x4 w = *(const u32x4*)(a.wo[br] + (size_t)row * 512 + cb * 8);
#pragma unroll
    for (int b = 0; b < 2; ++b) {
      const int hh = (br * 2 + b) * 32 + h;
      float M = -INFINITY;
      for (int s = 0; s < NSPLIT; ++s) M = fmaxf(M, a.pml[(hh * NSPLIT + s) * 2]);
      float L = 0.f, o = 0.f;
      for (int s = 0; s < NSPLIT; ++s) { float wt = __expf(a.pml[(hh * NSPLIT + s) * 2] - M); L += wt * a.pml[(hh * NSPLIT + s) * 2 + 1]; o += wt * a.po[((size_t)hh * NSPLIT + s) * 128 + d]; }
      o = rbf(o / L);
#pragma unroll
      for (int j = 0; j < 4; ++j) { acc[b][2 * j] += o * bflo(w[j]); acc[b][2 * j + 1] += o * bfhi(w[j]); }
    }
  }
#pragma unroll
  for (int b = 0; b < 2; ++b)
#pragma unroll
    for (int j = 0; j < 8; ++j) { float v = acc[b][j]; for (int o = 32; o; o >>= 1) v += __shfl_xor(v, o); if ((tid & 63) == 0) red[tid >> 6][b][j] = v; }
  __syncthreads();
  if (tid < 16) { int b = tid >> 3, j = tid & 7; float s = 0.f; for (int w = 0; w < 16; ++w) s += red[w][b][j];
    __hip_bfloat16 o = __float2bfloat16(s); a.out[b * 1024 + br * 512 + cb * 8 + j] = __builtin_bit_cast(uint16_t, o); }
}

extern "C" void kernel_launch(void* const* d_in, const int* in_sizes, int n_in, void* d_out, int out_size, void* d_ws, size_t ws_size, hipStream_t stream) {
  Args a;
  for (int i = 0; i < 2; ++i) {
    a.x[i] = (const uint16_t*)d_in[5 * i + 0]; a.k[i] = (const uint16_t*)d_in[5 * i + 1]; a.v[i] = (const uint16_t*)d_in[5 * i + 2];
    a.wq[i] = (const uint16_t*)d_in[5 * i + 3]; a.wo[i] = (const uint16_t*)d_in[5 * i + 4];
  }
  a.out = (uint16_t*)d_out; a.q = (float*)d_ws; a.po = a.q + 16384; a.pml = a.po + 128 * NSPLIT * 128;
  k_q<<<256, 256, 0, stream>>>(a);
  k_attn<<<256, 256, 0, stream>>>(a);
  k_o<<<128, 1024, 0, stream>>>(a);
}
```

```cpp
#define METRIC 0
#define PDEPTH 32
#define PAUX 1
#include <hip/hip_runtime.h>
#include <hip/hip_bf16.h>
#include <stdint.h>

typedef unsigned int u32;
typedef u32 u32x4 __attribute__((ext_vector_type(4)));
typedef float f32x4 __attribute__((ext_vector_type(4)));

constexpr int B = 2, D = 512, HQ = 32, HKV = 8, SK = 8192, DH = 128, W = 2048, G = 4;
constexpr int NSPLIT = 8;

__device__ __forceinline__ float bflo(u32 x) { return __builtin_bit_cast(float, x << 16); }
__device__ __forceinline__ float bfhi(u32 x) { return __builtin_bit_cast(float, x & 0xffff0000u); }
__device__ __forceinline__ float rbf(float f) { return __bfloat162float(__float2bfloat16(f)); }

struct Args {
  const uint16_t* x[2]; const uint16_t* k[2]; const uint16_t* v[2]; const uint16_t* wq[2]; const uint16_t* wo[2];
  uint16_t* out; float* q; float* po; float* pml;
};

__global__ void __launch_bounds__(256) k_q(Args a) {
  __shared__ float red[64][2][32];
  const int br = blockIdx.x >> 7, cb = blockIdx.x & 127;
  const int tid = threadIdx.x, r = tid >> 2, c = tid & 3;
  const uint16_t* wq = a.wq[br]; const uint16_t* x = a.x[br];
  float acc[2][8] = {};
  u32x4 w[8];
#pragma unroll
  for (int i = 0; i < 8; ++i) w[i] = *(const u32x4*)(wq + (size_t)(r + 64 * i) * 4096 + cb * 32 + c * 8);
#pragma unroll
  for (int i = 0; i < 8; ++i) {
    float x0 = bflo(x[r + 64 * i]), x1 = bflo(x[512 + r + 64 * i]);
#pragma unroll
    for (int j = 0; j < 4; ++j) {
      acc[0][2 * j] += x0 * bflo(w[i][j]); acc[0][2 * j + 1] += x0 * bfhi(w[i][j]);
      acc[1][2 * j] += x1 * bflo(w[i][j]); acc[1][2 * j + 1] += x1 * bfhi(w[i][j]);
    }
  }
#pragma unroll
  for (int j = 0; j < 8; ++j) { red[r][0][c * 8 + j] = acc[0][j]; red[r][1][c * 8 + j] = acc[1][j]; }
  __syncthreads();
  if (tid < 64) {
    int b = tid >> 5, col = tid & 31; float s = 0.f;
    for (int i = 0; i < 64; ++i) s += red[i][b][col];
    a.q[(br * 2 + b) * 4096 + cb * 32 + col] = rbf(s);
  }
}

__global__ void __launch_bounds__(256) k_attn(Args a) {
  __shared__ float qs[4][128];
  __shared__ float ps[4][256];
  __shared__ float red[16][4][128];
  __shared__ float wm[4][4], wl[4][4];
  const int blk = blockIdx.x, s = blk & 7, kvh = (blk >> 3) & 7, b = (blk >> 6) & 1, br = blk >> 7;
  const int tid = threadIdx.x;
  for (int i = tid; i < 512; i += 256) qs[i >> 7][i & 127] = a.q[(br * 2 + b) * 4096 + kvh * 512 + i];
  __syncthreads();
  const size_t base = ((size_t)(b * HKV + kvh) * SK + (SK - W) + s * 256) * DH;
  const uint16_t* kp = a.k[br] + base + (size_t)tid * DH;
  float sc[4] = {};
#pragma unroll 4
  for (int i = 0; i < 16; ++i) {
    u32x4 kk = *(const u32x4*)(kp + i * 8);
#pragma unroll
    for (int j = 0; j < 4; ++j)
#pragma unroll
      for (int g = 0; g < 4; ++g) sc[g] += qs[g][i * 8 + 2 * j] * bflo(kk[j]) + qs[g][i * 8 + 2 * j + 1] * bfhi(kk[j]);
  }
  float m[4];
#pragma unroll
  for (int g = 0; g < 4; ++g) { sc[g] *= 0.08838834764831845f; m[g] = sc[g];
    for (int o = 32; o; o >>= 1) m[g] = fmaxf(m[g], __shfl_xor(m[g], o));
    if ((tid & 63) == 0) wm[tid >> 6][g] = m[g]; }
  __syncthreads();
  float l[4];
#pragma unroll
  for (int g = 0; g < 4; ++g) { m[g] = fmaxf(fmaxf(wm[0][g], wm[1][g]), fmaxf(wm[2][g], wm[3][g]));
    float p = __expf(sc[g] - m[g]); ps[g][tid] = p; l[g] = p;
    for (int o = 32; o; o >>= 1) l[g] += __shfl_xor(l[g], o);
    if ((tid & 63) == 0) wl[tid >> 6][g] = l[g]; }
  __syncthreads();
  const int r = tid >> 4, c = tid & 15;
  float acc[4][8] = {};
  const uint16_t* vp = a.v[br] + base;
#pragma unroll 4
  for (int i = 0; i < 16; ++i) {
    u32x4 vv = *(const u32x4*)(vp + (size_t)(r + 16 * i) * DH + c * 8);
#pragma unroll
    for (int g = 0; g < 4; ++g) { float p = ps[g][r + 16 * i];
#pragma unroll
      for (int j = 0; j < 4; ++j) { acc[g][2 * j] += p * bflo(vv[j]); acc[g][2 * j + 1] += p * bfhi(vv[j]); } }
  }
#pragma unroll
  for (int g = 0; g < 4; ++g)
#pragma unroll
    for (int j = 0; j < 8; ++j) red[r][g][c * 8 + j] = acc[g][j];
  __syncthreads();
  for (int i = tid; i < 512; i += 256) { int g = i >> 7, d = i & 127; float sum = 0.f;
    for (int rr = 0; rr < 16; ++rr) sum += red[rr][g][d];
    a.po[((size_t)((br * 2 + b) * 32 + kvh * 4 + g) * NSPLIT + s) * 128 + d] = sum; }
  if (tid < 4) { int g = tid; float L = wl[0][g] + wl[1][g] + wl[2][g] + wl[3][g];
    float M = fmaxf(fmaxf(wm[0][g], wm[1][g]), fmaxf(wm[2][g], wm[3][g]));
    a.pml[(((br * 2 + b) * 32 + kvh * 4 + g) * NSPLIT + s) * 2] = M; a.pml[(((br * 2 + b) * 32 + kvh * 4 + g) * NSPLIT + s) * 2 + 1] = L; }
}

__global__ void __launch_bounds__(1024) k_o(Args a) {
  __shared__ float red[16][2][8];
  const int br = blockIdx.x >> 6, cb = blockIdx.x & 63, tid = threadIdx.x;
  float acc[2][8] = {};
#pragma unroll 1
  for (int i = 0; i < 4; ++i) {
    const int row = tid + 1024 * i, h = row >> 7, d = row & 127;
    u32x4 w = *(const u32x4*)(a.wo[br] + (size_t)row * 512 + cb * 8);
#pragma unroll
    for (int b = 0; b < 2; ++b) {
      const int hh = (br * 2 + b) * 32 + h;
      float M = -INFINITY;
      for (int s = 0; s < NSPLIT; ++s) M = fmaxf(M, a.pml[(hh * NSPLIT + s) * 2]);
      float L = 0.f, o = 0.f;
      for (int s = 0; s < NSPLIT; ++s) { float wt = __expf(a.pml[(hh * NSPLIT + s) * 2] - M); L += wt * a.pml[(hh * NSPLIT + s) * 2 + 1]; o += wt * a.po[((size_t)hh * NSPLIT + s) * 128 + d]; }
      o = rbf(o / L);
#pragma unroll
      for (int j = 0; j < 4; ++j) { acc[b][2 * j] += o * bflo(w[j]); acc[b][2 * j + 1] += o * bfhi(w[j]); }
    }
  }
#pragma unroll
  for (int b = 0; b < 2; ++b)
#pragma unroll
    for (int j = 0; j < 8; ++j) { float v = acc[b][j]; for (int o = 32; o; o >>= 1) v += __shfl_xor(v, o); if ((tid & 63) == 0) red[tid >> 6][b][j] = v; }
  __syncthreads();
  if (tid < 16) { int b = tid >> 3, j = tid & 7; float s = 0.f; for (int w = 0; w < 16; ++w) s += red[w][b][j];
    __hip_bfloat16 o = __float2bfloat16(s); a.out[b * 1024 + br * 512 + cb * 8 + j] = __builtin_bit_cast(uint16_t, o); }
}


typedef unsigned long long u64;
__device__ __forceinline__ u64 rt() { u64 t; asm volatile("s_memrealtime %0\n\ts_waitcnt lgkmcnt(0)" : "=s"(t) :: "memory"); return t; }
#define SB() __builtin_amdgcn_sched_barrier(0)
template <int AUX> __device__ __forceinline__ u32x4 ld(const void* p) {
  if (AUX == 1) return __builtin_nontemporal_load((const u32x4*)p); else return *(const u32x4*)p;
}
struct PArgs { const uint16_t* k[2]; const uint16_t* v[2]; const uint16_t* wq[2]; const uint16_t* wo[2]; u64* stamps; u32* sink; };
template <int THR, int DEPTH, int AUX>
__global__ void __launch_bounds__(THR) k_ring(PArgs a) {
  u64 t0 = rt(); SB();
  const int c0 = blockIdx.x, xcd = c0 & 7, idx = c0 >> 3, g = xcd * 2 + (idx >> 4), j = idx & 15;
  const int br = g >> 3, kvh = g & 7, b = j >> 3, s = j & 7;
  const int tid = threadIdx.x;
  constexpr int TT = THR;
  constexpr int NW = 32768 / 16 / TT, NK = 65536 / 16 / TT, NTOT = 2 * NW + 2 * NK;
  const uint16_t* wq = a.wq[br] + kvh * 512 + j * 32; const uint16_t* wo = a.wo[br] + (size_t)(kvh * 512 + j * 32) * 512;
  const size_t kvbase = ((size_t)(b * 8 + kvh) * 8192 + 6144 + s * 256) * 128;
  const uint16_t* kp = a.k[br] + kvbase; const uint16_t* vp = a.v[br] + kvbase;
  asm volatile("" :: "s"(wq), "s"(wo), "s"(kp), "s"(vp));
  u64 t1 = rt(); SB();
  u32x4 r[DEPTH];
  u32 acc = 0;
  u64 tf = 0;
  auto issue = [&](int i) -> u32x4 {
    if (i < NW) { int e = tid + i * TT; return ld<AUX>(wq + (size_t)(e >> 2) * 4096 + (e & 3) * 8); }
    else if (i < NW + NK) { int e = tid + (i - NW) * TT; return ld<AUX>(kp + e * 8); }
    else if (i < NW + 2 * NK) { int e = tid + (i - NW - NK) * TT; return ld<AUX>(vp + e * 8); }
    else { int e = tid + (i - NW - 2 * NK) * TT; return ld<AUX>(wo + e * 8); }
  };
#pragma unroll
  for (int i = 0; i < DEPTH && i < NTOT; ++i) r[i] = issue(i);
  SB();
#pragma unroll
  for (int i = 0; i < NTOT; ++i) {
    u32x4 cur = r[i % DEPTH];
    acc ^= cur[0] ^ cur[1] ^ cur[2] ^ cur[3];
    asm volatile("" : "+v"(acc)); SB();
    if (i == 0) { tf = rt(); SB(); }
    if (i + DEPTH < NTOT) r[i % DEPTH] = issue(i + DEPTH);
    SB();
  }
  u64 t6 = rt(); SB();
  if (acc == 0x12345678u) a.sink[tid] = acc;
  if ((threadIdx.x & 63) == 0) {
    u64* st = a.stamps + ((size_t)c0 * 4 + (threadIdx.x >> 6)) * 4;
    st[0] = t0; st[1] = t1; st[2] = tf; st[3] = t6;
  }
}
__global__ void k_enc(const u64* st) {
  u64 t0 = rt();
  u64 mins = ~0ull, maxs = 0, maxe = 0, sumk = 0, sumd = 0, s5 = 0, s0 = 0, e0 = 0, minf = ~0ull;
  for (int c = 0; c < 256; ++c) for (int w = 0; w < 4; ++w) { const u64* p = st + (c * 4 + w) * 4;
    mins = p[0] < mins ? p[0] : mins; maxs = p[0] > maxs ? p[0] : maxs; maxe = p[3] > maxe ? p[3] : maxe; sumk += p[1] - p[0]; sumd += p[3] - p[0];
    minf = (p[2] - p[1]) < minf ? (p[2] - p[1]) : minf;
    if (c == 5 && w == 0) s5 = p[0]; if (c == 0 && w == 0) { s0 = p[0]; e0 = p[3]; } }
  u64 m = 0;
  if (METRIC == 1) m = maxs - mins;
  if (METRIC == 2) m = maxe - mins;
  if (METRIC == 3) m = sumd / 1024;
  if (METRIC == 4) m = sumk / 1024;
  if (METRIC == 5) m = s5 - mins;
  if (METRIC == 6) m = s0 - mins;
  if (METRIC == 7) m = minf;
  if (m > 5000) m = 5000;
  while (rt() - t0 < 3000 + m * 20) {}
}
extern "C" void kernel_launch(void* const* d_in, const int* in_sizes, int n_in, void* d_out, int out_size, void* d_ws, size_t ws_size, hipStream_t stream) {
  Args a; PArgs p;
  for (int i = 0; i < 2; ++i) {
    a.x[i] = (const uint16_t*)d_in[5 * i + 0]; a.k[i] = (const uint16_t*)d_in[5 * i + 1]; a.v[i] = (const uint16_t*)d_in[5 * i + 2];
    a.wq[i] = (const uint16_t*)d_in[5 * i + 3]; a.wo[i] = (const uint16_t*)d_in[5 * i + 4];
    p.k[i] = a.k[i]; p.v[i] = a.v[i]; p.wq[i] = a.wq[i]; p.wo[i] = a.wo[i];
  }
  a.out = (uint16_t*)d_out; a.q = (float*)d_ws; a.po = a.q + 16384; a.pml = a.po + 128 * NSPLIT * 128;
  p.stamps = (u64*)((char*)d_ws + (16 << 20)); p.sink = (u32*)((char*)d_ws + (17 << 20));
  k_ring<256, PDEPTH, PAUX><<<256, 256, 0, stream>>>(p);
  k_q<<<256, 256, 0, stream>>>(a);
  k_attn<<<256, 256, 0, stream>>>(a);
  k_o<<<128, 1024, 0, stream>>>(a);
  k_enc<<<1, 64, 0, stream>>>(p.stamps);
}
```

```cpp
#include <hip/hip_runtime.h>
#include <hip/hip_bf16.h>
#include <stdint.h>

typedef unsigned int u32;
typedef unsigned long long u64;
typedef u32 u32x4 __attribute__((ext_vector_type(4)));
typedef u32 u32x2 __attribute__((ext_vector_type(2)));
typedef u32 u32x16 __attribute__((ext_vector_type(16)));
typedef float f32x4 __attribute__((ext_vector_type(4)));
typedef short bf16x8 __attribute__((ext_vector_type(8)));
#define LAS __attribute__((address_space(3)))
#define CAS __attribute__((address_space(4)))
#define GAS __attribute__((address_space(1)))

#define STAMPS 0
#define QSLEEP 24
#define QPOS 15
#define STICKY 1
#define OSLEEP 0
#define OLEAD 0
#define XQ 2
#define XO 2
#define PREF 1

constexpr int SK = 8192, WND = 2048;
constexpr int AUX_NT = 2, AUX_SC1 = 16;
constexpr int WS_EPOCH = 0;
constexpr int WS_Q = 4096;
constexpr int WS_ML = WS_Q + 131072;
constexpr int WS_O = WS_ML + 32768;
constexpr int WS_Y = WS_O + 524288;
constexpr int WS_END = WS_Y + 262144;
constexpr int WS_STAMP = 4 << 20;
constexpr u32 SPIN_CAP = 1u << 18;

struct Branch { const void* wq; const void* x; const void* k; const void* v; const void* wo; void* ws; void* out; void* ws2; };
struct Params { Branch br[2]; };

__device__ u32x4 raw_ld128(u32x4 rsrc, int voff, int soff, int aux) __asm("llvm.amdgcn.raw.buffer.load.v4i32");
__device__ u32 raw_ld32(u32x4 rsrc, int voff, int soff, int aux) __asm("llvm.amdgcn.raw.buffer.load.i32");
__device__ void raw_st128(u32x4 v, u32x4 rsrc, int voff, int soff, int aux) __asm("llvm.amdgcn.raw.buffer.store.v4i32");
__device__ void raw_st64(u32x2 v, u32x4 rsrc, int voff, int soff, int aux) __asm("llvm.amdgcn.raw.buffer.store.v2i32");
__device__ void raw_st32(u32 v, u32x4 rsrc, int voff, int soff, int aux) __asm("llvm.amdgcn.raw.buffer.store.i32");
typedef u32x4 rsrc_t;
__device__ __forceinline__ rsrc_t mk(u32 lo, u32 hi, u32 bytes) { return rsrc_t{lo, hi, bytes, 0x00020000u}; }
template <int AUX> __device__ __forceinline__ u32x4 ld16(rsrc_t r, int voff, int soff) { return raw_ld128(r, voff, soff, AUX); }
__device__ __forceinline__ f32x4 mfma(u32x4 a, u32x4 b, f32x4 c) {
  return __builtin_amdgcn_mfma_f32_16x16x32_bf16(__builtin_bit_cast(bf16x8, a), __builtin_bit_cast(bf16x8, b), c, 0, 0, 0);
}
__device__ __forceinline__ u32 f2u(float f) { return __builtin_bit_cast(u32, f); }
__device__ __forceinline__ float u2f(u32 u) { return __builtin_bit_cast(float, u); }
template <int CTRL> __device__ __forceinline__ u32 dppu(u32 x) { return (u32)__builtin_amdgcn_mov_dpp((int)x, CTRL, 0xf, 0xf, true); }
template <int CTRL> __device__ __forceinline__ float dpp(float x) { return u2f(dppu<CTRL>(f2u(x))); }
typedef __bf16 bf16x2_t __attribute__((ext_vector_type(2)));
typedef float f32x2 __attribute__((ext_vector_type(2)));
__device__ __forceinline__ u32 pkbf(float lo, float hi) { f32x2 v = {lo, hi}; return __builtin_bit_cast(u32, __builtin_convertvector(v, bf16x2_t)); }
__device__ __forceinline__ float x16_max(float x) { auto s = __builtin_amdgcn_permlane16_swap(f2u(x), f2u(x), false, false); return fmaxf(u2f(s[0]), u2f(s[1])); }
__device__ __forceinline__ float x32_max(float x) { auto s = __builtin_amdgcn_permlane32_swap(f2u(x), f2u(x), false, false); return fmaxf(u2f(s[0]), u2f(s[1])); }
__device__ __forceinline__ float t16_sum(float a, float b) { auto s = __builtin_amdgcn_permlane16_swap(f2u(a), f2u(b), false, false); return u2f(s[0]) + u2f(s[1]); }
__device__ __forceinline__ float t32_sum(float a, float b) { auto s = __builtin_amdgcn_permlane32_swap(f2u(a), f2u(b), false, false); return u2f(s[0]) + u2f(s[1]); }
#define SB() __builtin_amdgcn_sched_barrier(0)
#define STR2(x) #x
#define STR(x) STR2(x)
#define VMW(n) do { SB(); asm volatile("s_waitcnt vmcnt(" STR(n) ")" ::: "memory"); SB(); } while (0)
#define BAR() do { SB(); asm volatile("s_waitcnt lgkmcnt(0)\n\ts_barrier" ::: "memory"); SB(); } while (0)
#define PIN(x) asm volatile("" : "+v"(x))

__device__ __forceinline__ u32x4 tag4(f32x4 v, u32 tag) {
  u32x4 r;
  r[0] = __builtin_amdgcn_perm(f2u(v[0]), tag, 0x07060500u); r[1] = __builtin_amdgcn_perm(f2u(v[1]), tag, 0x07060501u);
  r[2] = __builtin_amdgcn_perm(f2u(v[2]), tag, 0x07060502u); r[3] = __builtin_amdgcn_perm(f2u(v[3]), tag, 0x07060503u);
  return r;
}
__device__ __forceinline__ u32 miss(u32x4 v, u32 tag) {
  const u32 a = __builtin_amdgcn_perm(v[1], v[0], 0x0c0c0400u), b = __builtin_amdgcn_perm(v[3], v[2], 0x04000c0cu);
  return (a | b) ^ tag;
}
__device__ __forceinline__ float val(u32 u) { return u2f((u & 0xffffff00u) | 0x80u); }
__device__ __forceinline__ void tr_pair(const u32x4& r0, const u32x4& r1, u32x4 (&f)[8], int k) {
#pragma unroll
  for (int d = 0; d < 4; ++d) { f[2 * d][k] = __builtin_amdgcn_perm(r1[d], r0[d], 0x05040100u); f[2 * d + 1][k] = __builtin_amdgcn_perm(r1[d], r0[d], 0x07060302u); }
}
#define STAMP(i) do {} while (0)

constexpr int L_QP = 0;
constexpr int L_QF = 4096;
constexpr int L_TAG = 5120;
constexpr int L_ML = 5248;
constexpr int L_OP = 5632;
constexpr int L_YP = L_OP + 8192;
constexpr int L_END = L_YP + 4096;

__global__ void __launch_bounds__(320, 2) fused_decode(Params p) {
  __shared__ __attribute__((aligned(16))) unsigned char smem[L_END];
  LAS unsigned char* L = (LAS unsigned char*)smem;
  STAMP(0);
  const u32 c = blockIdx.x, idx = c >> 3, grp = (c & 7) * 2 + (idx >> 4), j = idx & 15;
  const u32 br = grp >> 3, kvh = grp & 7;
  const u32 gq = grp ^ XQ, kq = gq & 7, go = grp ^ XO, ko = go & 7;
  const u32x16 ka = *(const CAS u32x16*)((const CAS char*)__builtin_amdgcn_kernarg_segment_ptr() + br * 64);
  const u32 tid = threadIdx.x, l = tid & 63, w = __builtin_amdgcn_readfirstlane(tid >> 6);
  const u32 g = l >> 4, i16 = l & 15;
  const u32 hh = j >> 2, cb = j & 3;
  constexpr float C2 = 0.08838834764831845f * 1.4426950408889634f;
  const f32x4 z = {0.f, 0.f, 0.f, 0.f};

  if (__builtin_expect(w < 4, 1)) {
    const u32 qc = j & 7, qh = j >> 3, b = j >> 3, s = j & 7;
    int oq = (qh * 256 + 64 * w + 8 * (l >> 3)) * 8192 + (kq * 512 + qc * 64) * 2 + (l & 7) * 16;
    int ox = ((i16 >> 3) * 512 + qh * 256 + 64 * w + 8 * (2 * g + ((i16 >> 2) & 1))) * 2;
    PIN(oq); PIN(ox); SB();
    const rsrc_t rq = mk(ka[0], ka[1], 512 * 4096 * 2), rx = mk(ka[2], ka[3], 2048), rk = mk(ka[4], ka[5], 2 * 8 * SK * 256), rv = mk(ka[6], ka[7], 2 * 8 * SK * 256),
                 ro = mk(ka[8], ka[9], 4096 * 512 * 2), rws = mk(ka[10], ka[11], WS_END), rout = mk(ka[12], ka[13], 4096);
    u32x4 X, Wq[8], K[16], V[16], Wo[8], F[8];
#pragma unroll
    for (int t = 0; t < 8; ++t) Wq[t] = ld16<AUX_NT>(rq, oq, t * 8192);
    X = ld16<0>(rx, ox, 0);
    SB();
    STAMP(1);
    const int kv0 = ((b * 8 + kvh) * SK + (SK - WND) + s * 256) * 256 + w * 1024;
    int ok = kv0 + ((l >> 2) & 3) * 256 + g * 64 + (l & 3) * 16;
    int ov = kv0 + l * 16;
    int oo = (ko * 512 + hh * 128 + 32 * w + 2 * g) * 1024 + cb * 256 + i16 * 16;
    const u32 prod = ((l >> 2) & 1) * 8 + (l >> 3);
    int po = WS_O + (go * 16 + prod) * 2048 + (hh * 16 + 4 * w + (l & 3)) * 16;
    int pm = WS_ML + (go * 16 + prod) * 128 + hh * 16;
    int lq = L_QP + w * 1024 + (g * 8 + (l & 7)) * 16;
    int ly = L_YP + w * 1024 + ((g & 1) * 32 + i16 * 2 + (g >> 1)) * 16;
    PIN(ok); PIN(ov); PIN(oo); PIN(po); PIN(pm); PIN(lq); PIN(ly);
#pragma unroll
    for (int t = 0; t < 8; ++t) { VMW(8); K[t] = ld16<AUX_NT>(rk, ok, t * 4096); SB(); if (t & 1) tr_pair(Wq[t - 1], Wq[t], F, t >> 1); }
    SB();
    K[8] = ld16<AUX_NT>(rk, ok, 8 * 4096);
    SB();
    STAMP(2);
    {
      f32x4 d[8];
#pragma unroll
      for (int e = 0; e < 8; ++e) d[e] = mfma(X, F[e], z);
      SB();
      if ((g & 1) == ((l >> 3) & 1)) {
        *(LAS f32x4*)(L + lq) = f32x4{d[0][0], d[1][0], d[2][0], d[3][0]};
        *(LAS f32x4*)(L + lq + 512) = f32x4{d[4][0], d[5][0], d[6][0], d[7][0]};
      }
    }
    BAR();
#pragma unroll
    for (int t = 9; t < 16; ++t) { VMW(7); K[t] = ld16<AUX_NT>(rk, ok, t * 4096); }
#pragma unroll
    for (int t = 0; t <= QPOS; ++t) { if (t < QPOS - 1) VMW(7); V[t] = ld16<AUX_NT>(rv, ov, t * 4096); }
    STAMP(3);
    BAR();
    STAMP(4);
    const u32x4 Q = *(const LAS u32x4*)(L + L_QF + l * 16);
    const u32 tag = *(const LAS u32*)(L + L_TAG);
    float sc[16];
    {
      f32x4 d[16];
#pragma unroll
      for (int t = 0; t < 16; ++t) d[t] = mfma(K[t], Q, z);
      SB();
#pragma unroll
      for (int t = 0; t < 16; ++t) sc[t] = d[t][0] + dpp<0x55>(d[t][1]);
      SB();
#pragma unroll
      for (int t = 0; t < 16; ++t) sc[t] += dpp<0xAA>(d[t][2]);
      SB();
#pragma unroll
      for (int t = 0; t < 16; ++t) sc[t] += dpp<0xFF>(d[t][3]);
      SB();
    }
    float m = sc[0];
#pragma unroll
    for (int t = 1; t < 16; ++t) m = fmaxf(m, sc[t]);
    m = x32_max(x16_max(m)) * C2;
    float lsum = 0.f;
#pragma unroll
    for (int t = 0; t < 16; ++t) { sc[t] = __builtin_amdgcn_exp2f(fmaf(sc[t], C2, -m)); lsum += sc[t]; }
    lsum = t16_sum(lsum, lsum); lsum = t32_sum(lsum, lsum);
    if (l < 16 && (l & 3) == 0) *(LAS f32x2*)(L + L_ML + (w * 4 + (l >> 2)) * 8) = f32x2{m, lsum};
    u32x4 P0, P1;
#pragma unroll
    for (int k = 0; k < 4; ++k) { P0[k] = pkbf(sc[2 * k], sc[2 * k + 1]); P1[k] = pkbf(sc[8 + 2 * k], sc[9 + 2 * k]); }
    BAR();
    STAMP(5);
#pragma unroll
    for (int t = QPOS + 1; t < 16; ++t) { VMW(7); V[t] = ld16<AUX_NT>(rv, ov, t * 4096); }
    f32x4 acc[8];
    VMW(7); Wo[0] = ld16<AUX_NT>(ro, oo, 0);
    VMW(7); Wo[1] = ld16<AUX_NT>(ro, oo + 1024, 0); SB();
#pragma unroll
    for (int k = 0; k < 4; ++k) tr_pair(V[2 * k], V[2 * k + 1], F, k);
#pragma unroll
    for (int e = 0; e < 8; ++e) acc[e] = mfma(P0, F[e], z);
    SB();
    tr_pair(V[8], V[9], F, 0);
#pragma unroll
    for (int t = 2; t < 8; ++t) { VMW(7); Wo[t] = ld16<AUX_NT>(ro, oo + (t & 1) * 1024, (t >> 1) * 8192); SB(); if (t & 1) tr_pair(V[7 + t], V[8 + t], F, t >> 1); }
    STAMP(6);
#pragma unroll
    for (int e = 0; e < 8; ++e) acc[e] = mfma(P1, F[e], acc[e]);
    SB();
    *(LAS f32x4*)(L + L_OP + w * 2048 + l * 16) = f32x4{acc[0][0], acc[1][0], acc[2][0], acc[3][0]};
    *(LAS f32x4*)(L + L_OP + w * 2048 + 1024 + l * 16) = f32x4{acc[4][0], acc[5][0], acc[6][0], acc[7][0]};
    BAR();
    u32x4 ml = ld16<AUX_SC1>(rws, pm, 0);
    SB();
#pragma unroll
    for (int k = 0; k < 4; ++k) tr_pair(Wo[2 * k], Wo[2 * k + 1], F, k);
#pragma unroll
    for (int e = 0; e < 8; ++e) PIN(F[e]);
    SB();
    if (OLEAD) __builtin_amdgcn_s_sleep(OLEAD);
    u32x4 o0 = ld16<AUX_SC1>(rws, po, 0), o1 = ld16<AUX_SC1>(rws, po + 1024, 0);
    SB();
    STAMP(7);
#pragma unroll 1
    for (u32 spins = 0; spins < SPIN_CAP; ++spins) {
      const bool ms_ = miss(ml, tag) != 0;
      if (__builtin_amdgcn_ballot_w64(ms_) == 0) break;
      asm volatile("" ::: "memory");
      if (OSLEEP) __builtin_amdgcn_s_sleep(OSLEEP);
      if (!STICKY || ms_) ml = ld16<AUX_SC1>(rws, pm, 0);
    }
    float f;
    {
      const float ms = u2f(ml[0] & 0xffffff00u) + val(ml[1]), ls = val(ml[2]);
      float M = fmaxf(ms, dpp<0x128>(ms)); M = x32_max(x16_max(M));
      const float wt = __builtin_amdgcn_exp2f(ms - M);
      float Ls = wt * ls; Ls += dpp<0x128>(Ls); Ls = t16_sum(Ls, Ls); Ls = t32_sum(Ls, Ls);
      f = wt * __builtin_amdgcn_rcpf(Ls);
    }
    PIN(f);
    STAMP(2);
#pragma unroll 1
    for (u32 spins = 0; spins < SPIN_CAP; ++spins) {
      const bool ms_ = (miss(o0, tag) | miss(o1, tag)) != 0;
      if (__builtin_amdgcn_ballot_w64(ms_) == 0) break;
      asm volatile("" ::: "memory");
      if (OSLEEP) __builtin_amdgcn_s_sleep(OSLEEP);
      if (!STICKY || ms_) { o0 = ld16<AUX_SC1>(rws, po, 0); o1 = ld16<AUX_SC1>(rws, po + 1024, 0); }
    }
    STAMP(8);
    {
      float a[4];
#pragma unroll
      for (int e = 0; e < 4; ++e) a[e] = t32_sum(val(o0[e]) * f, val(o1[e]) * f);
      float b0 = t16_sum(a[0], a[2]), b1 = t16_sum(a[1], a[3]);
      b0 += dpp<0x128>(b0); b1 += dpp<0x128>(b1);
      const u32 pk = pkbf(b0, b1);
      const u32x4 A = {pk, dppu<0x55>(pk), dppu<0xAA>(pk), dppu<0xFF>(pk)};
      f32x4 d[8];
#pragma unroll
      for (int e = 0; e < 8; ++e) d[e] = mfma(A, F[e], z);
      SB();
      f32x4 y;
#pragma unroll
      for (int e = 0; e < 4; ++e) y[e] = (g >> 1) ? d[e + 4][0] : d[e][0];
      *(LAS f32x4*)(L + ly) = y;
    }
    BAR();
    STAMP(9);
    if (w == 0 && hh == 0) {
      const int py = WS_Y + (((br * 4 + cb) * 32 + (l >> 3)) * 64 + kvh * 8 + (l & 7)) * 16;
      u32x4 y0, y1, y2, y3;
#pragma unroll 1
      for (u32 spins = 0; spins < SPIN_CAP; ++spins) {
        y0 = ld16<AUX_SC1>(rws, py, 0); y1 = ld16<AUX_SC1>(rws, py, 8192); y2 = ld16<AUX_SC1>(rws, py, 16384); y3 = ld16<AUX_SC1>(rws, py, 24576);
        if (__builtin_amdgcn_ballot_w64((miss(y0, tag) | miss(y1, tag) | miss(y2, tag) | miss(y3, tag)) != 0) == 0) break;
        asm volatile("" ::: "memory");
      }
      STAMP(10);
      float y[4];
#pragma unroll
      for (int e = 0; e < 4; ++e) y[e] = (val(y0[e]) + val(y1[e])) + (val(y2[e]) + val(y3[e]));
      float b0 = t32_sum(y[0], y[2]), b1 = t32_sum(y[1], y[3]);
      b0 = t16_sum(b0, b0); b1 = t16_sum(b1, b1);
      b0 += dpp<0x128>(b0); b1 += dpp<0x128>(b1);
      const u32 u = kvh * 8 + (l & 7);
      if ((l & 24) == 0) raw_st32(pkbf(b0, b1), rout, ((u >> 5) * 1024 + br * 512 + cb * 128 + 4 * (u & 31) + 2 * (l >> 5)) * 2, 0, 0);
      if (kvh == 0 && cb == 0 && l == 0) raw_st32(tag, rws, WS_EPOCH + br * 256, 0, AUX_SC1);
    }
  } else {
    const u32 qc = j & 7, qh = j >> 3, b = j >> 3;
    const rsrc_t rws = mk(ka[10], ka[11], WS_END);
    const u32 ep = raw_ld32(rws, WS_EPOCH + br * 256, 0, AUX_SC1);
    int lqr = L_QP + ((l >> 3) & 1) * 512 + ((((l >> 4) & 1) * 2 + (l >> 5)) * 8 + (l & 7)) * 16;
    int sq = WS_Q + ((gq * 2 + ((l >> 4) & 1)) * 2 + qh) * 2048 + ((l >> 3) & 1) * 1024 + (qc * 8 + (l & 7)) * 16;
    int pq = WS_Q + (grp * 2 + b) * 4096 + ((i16 >> 2) * 16 + 4 * g + (l & 3)) * 16;
    int sm = WS_ML + (grp * 16 + j) * 128 + l * 16;
    int so = WS_O + (grp * 16 + j) * 2048 + l * 16;
    int sy = WS_Y + (((br * 4 + cb) * 32 + kvh * 4 + hh) * 64 + l) * 16;
    PIN(lqr); PIN(sq); PIN(pq); PIN(sm); PIN(so); PIN(sy);
    const u32 tag = __builtin_amdgcn_readfirstlane(((ep & 0x7f7f7f7fu) + 0x01010101u) ^ (ep & 0x80808080u));
    if (l == 0) *(LAS u32*)(L + L_TAG) = tag;
    STAMP(1);
    BAR();
    STAMP(2);
    {
      f32x4 sum = *(const LAS f32x4*)(L + lqr);
#pragma unroll
      for (int ww = 1; ww < 4; ++ww) sum += *(const LAS f32x4*)(L + lqr + ww * 1024);
#pragma unroll
      for (int e = 0; e < 4; ++e) sum[e] = t32_sum(sum[e], sum[e]);
      if (l < 32) raw_st128(tag4(sum, tag), rws, sq, 0, AUX_SC1);
    }
    STAMP(3);
    if (QSLEEP) __builtin_amdgcn_s_sleep(QSLEEP);
    {
      u32x4 a0, a1, b0, b1;
#pragma unroll 1
      for (u32 spins = 0; spins < SPIN_CAP; ++spins) {
        a0 = ld16<AUX_SC1>(rws, pq, 0); a1 = ld16<AUX_SC1>(rws, pq + 1024, 0); b0 = ld16<AUX_SC1>(rws, pq + 2048, 0); b1 = ld16<AUX_SC1>(rws, pq + 3072, 0);
        if (__builtin_amdgcn_ballot_w64((miss(a0, tag) | miss(a1, tag) | miss(b0, tag) | miss(b1, tag)) != 0) == 0) break;
        asm volatile("" ::: "memory");
      }
      u32x4 Q;
      Q[0] = pkbf(val(a0[0]) + val(b0[0]), val(a0[1]) + val(b0[1])); Q[1] = pkbf(val(a0[2]) + val(b0[2]), val(a0[3]) + val(b0[3]));
      Q[2] = pkbf(val(a1[0]) + val(b1[0]), val(a1[1]) + val(b1[1])); Q[3] = pkbf(val(a1[2]) + val(b1[2]), val(a1[3]) + val(b1[3]));
      *(LAS u32x4*)(L + L_QF + l * 16) = Q;
    }
    STAMP(4);
    BAR();
    BAR();
    STAMP(5);
    float fw[4];
    {
      f32x2 ml[4]; float mg[4];
#pragma unroll
      for (int ww = 0; ww < 4; ++ww) { ml[ww] = *(const LAS f32x2*)(L + L_ML + (ww * 4 + (l & 3)) * 8); mg[ww] = *(const LAS float*)(L + L_ML + (ww * 4 + g) * 8); }
      const float M = fmaxf(fmaxf(ml[0][0], ml[1][0]), fmaxf(ml[2][0], ml[3][0]));
      float Ls = 0.f;
#pragma unroll
      for (int ww = 0; ww < 4; ++ww) Ls = fmaf(__builtin_amdgcn_exp2f(ml[ww][0] - M), ml[ww][1], Ls);
      const float Mh = u2f(f2u(M) & 0xffffff00u);
      const f32x4 rec = {Mh, M - Mh, Ls, 0.f};
      if (l < 8) raw_st128(tag4(rec, tag), rws, sm, 0, AUX_SC1);
      const float Mg = fmaxf(fmaxf(mg[0], mg[1]), fmaxf(mg[2], mg[3]));
#pragma unroll
      for (int ww = 0; ww < 4; ++ww) { fw[ww] = __builtin_amdgcn_exp2f(mg[ww] - Mg); PIN(fw[ww]); }
    }
    BAR();
    STAMP(6);
    {
      f32x4 lo[4], hi[4];
#pragma unroll
      for (int ww = 0; ww < 4; ++ww) lo[ww] = *(const LAS f32x4*)(L + L_OP + ww * 2048 + l * 16);
#pragma unroll
      for (int ww = 0; ww < 4; ++ww) hi[ww] = *(const LAS f32x4*)(L + L_OP + ww * 2048 + 1024 + l * 16);
      SB();
      raw_st128(tag4(fw[0] * lo[0] + fw[1] * lo[1] + fw[2] * lo[2] + fw[3] * lo[3], tag), rws, so, 0, AUX_SC1);
      SB();
      raw_st128(tag4(fw[0] * hi[0] + fw[1] * hi[1] + fw[2] * hi[2] + fw[3] * hi[3], tag), rws, so + 1024, 0, AUX_SC1);
    }
    STAMP(7);
    BAR();
    STAMP(8);
    {
      f32x4 y = *(const LAS f32x4*)(L + L_YP + l * 16);
#pragma unroll
      for (int ww = 1; ww < 4; ++ww) y += *(const LAS f32x4*)(L + L_YP + ww * 1024 + l * 16);
      raw_st128(tag4(y, tag), rws, sy, 0, AUX_SC1);
    }
    STAMP(9);
  }
}

extern "C" void kernel_launch(void* const* d_in, const int* in_sizes, int n_in, void* d_out, int out_size, void* d_ws, size_t ws_size, hipStream_t stream) {
  Params p;
  for (int i = 0; i < 2; ++i) {
    p.br[i].x = d_in[5 * i + 0]; p.br[i].k = d_in[5 * i + 1]; p.br[i].v = d_in[5 * i + 2]; p.br[i].wq = d_in[5 * i + 3]; p.br[i].wo = d_in[5 * i + 4];
    p.br[i].ws = d_ws; p.br[i].out = d_out; p.br[i].ws2 = d_ws;
  }
  fused_decode<<<256, 320, 0, stream>>>(p);
}
```

```cpp
#define STAMPS 1
#define KIND 2
#define IDX 11
#define STAT 0
#include <hip/hip_runtime.h>
#include <hip/hip_bf16.h>
#include <stdint.h>

typedef unsigned int u32;
typedef unsigned long long u64;
typedef u32 u32x4 __attribute__((ext_vector_type(4)));
typedef u32 u32x2 __attribute__((ext_vector_type(2)));
typedef u32 u32x16 __attribute__((ext_vector_type(16)));
typedef float f32x4 __attribute__((ext_vector_type(4)));
typedef short bf16x8 __attribute__((ext_vector_type(8)));
#define LAS __attribute__((address_space(3)))
#define CAS __attribute__((address_space(4)))
#define GAS __attribute__((address_space(1)))

#define QSLEEP 24
#define QPOS 15
#define STICKY 1
#define OSLEEP 0
#define OLEAD 0
#define XQ 2
#define XO 2
#define PREF 1

constexpr int SK = 8192, WND = 2048;
constexpr int AUX_NT = 2, AUX_SC1 = 16;
constexpr int WS_EPOCH = 0;
constexpr int WS_Q = 4096;
constexpr int WS_ML = WS_Q + 131072;
constexpr int WS_O = WS_ML + 32768;
constexpr int WS_Y = WS_O + 524288;
constexpr int WS_END = WS_Y + 262144;
constexpr int WS_STAMP = 4 << 20;
constexpr u32 SPIN_CAP = 1u << 18;

struct Branch { const void* wq; const void* x; const void* k; const void* v; const void* wo; void* ws; void* out; void* ws2; };
struct Params { Branch br[2]; };

__device__ u32x4 raw_ld128(u32x4 rsrc, int voff, int soff, int aux) __asm("llvm.amdgcn.raw.buffer.load.v4i32");
__device__ u32 raw_ld32(u32x4 rsrc, int voff, int soff, int aux) __asm("llvm.amdgcn.raw.buffer.load.i32");
__device__ void raw_st128(u32x4 v, u32x4 rsrc, int voff, int soff, int aux) __asm("llvm.amdgcn.raw.buffer.store.v4i32");
__device__ void raw_st64(u32x2 v, u32x4 rsrc, int voff, int soff, int aux) __asm("llvm.amdgcn.raw.buffer.store.v2i32");
__device__ void raw_st32(u32 v, u32x4 rsrc, int voff, int soff, int aux) __asm("llvm.amdgcn.raw.buffer.store.i32");
typedef u32x4 rsrc_t;
__device__ __forceinline__ rsrc_t mk(u32 lo, u32 hi, u32 bytes) { return rsrc_t{lo, hi, bytes, 0x00020000u}; }
template <int AUX> __device__ __forceinline__ u32x4 ld16(rsrc_t r, int voff, int soff) { return raw_ld128(r, voff, soff, AUX); }
__device__ __forceinline__ f32x4 mfma(u32x4 a, u32x4 b, f32x4 c) {
  return __builtin_amdgcn_mfma_f32_16x16x32_bf16(__builtin_bit_cast(bf16x8, a), __builtin_bit_cast(bf16x8, b), c, 0, 0, 0);
}
__device__ __forceinline__ u32 f2u(float f) { return __builtin_bit_cast(u32, f); }
__device__ __forceinline__ float u2f(u32 u) { return __builtin_bit_cast(float, u); }
template <int CTRL> __device__ __forceinline__ u32 dppu(u32 x) { return (u32)__builtin_amdgcn_mov_dpp((int)x, CTRL, 0xf, 0xf, true); }
template <int CTRL> __device__ __forceinline__ float dpp(float x) { return u2f(dppu<CTRL>(f2u(x))); }
typedef __bf16 bf16x2_t __attribute__((ext_vector_type(2)));
typedef float f32x2 __attribute__((ext_vector_type(2)));
__device__ __forceinline__ u32 pkbf(float lo, float hi) { f32x2 v = {lo, hi}; return __builtin_bit_cast(u32, __builtin_convertvector(v, bf16x2_t)); }
__device__ __forceinline__ float x16_max(float x) { auto s = __builtin_amdgcn_permlane16_swap(f2u(x), f2u(x), false, false); return fmaxf(u2f(s[0]), u2f(s[1])); }
__device__ __forceinline__ float x32_max(float x) { auto s = __builtin_amdgcn_permlane32_swap(f2u(x), f2u(x), false, false); return fmaxf(u2f(s[0]), u2f(s[1])); }
__device__ __forceinline__ float t16_sum(float a, float b) { auto s = __builtin_amdgcn_permlane16_swap(f2u(a), f2u(b), false, false); return u2f(s[0]) + u2f(s[1]); }
__device__ __forceinline__ float t32_sum(float a, float b) { auto s = __builtin_amdgcn_permlane32_swap(f2u(a), f2u(b), false, false); return u2f(s[0]) + u2f(s[1]); }
#define SB() __builtin_amdgcn_sched_barrier(0)
#define STR2(x) #x
#define STR(x) STR2(x)
#define VMW(n) do { SB(); asm volatile("s_waitcnt vmcnt(" STR(n) ")" ::: "memory"); SB(); } while (0)
#define BAR() do { SB(); asm volatile("s_waitcnt lgkmcnt(0)\n\ts_barrier" ::: "memory"); SB(); } while (0)
#define PIN(x) asm volatile("" : "+v"(x))

__device__ __forceinline__ u32x4 tag4(f32x4 v, u32 tag) {
  u32x4 r;
  r[0] = __builtin_amdgcn_perm(f2u(v[0]), tag, 0x07060500u); r[1] = __builtin_amdgcn_perm(f2u(v[1]), tag, 0x07060501u);
  r[2] = __builtin_amdgcn_perm(f2u(v[2]), tag, 0x07060502u); r[3] = __builtin_amdgcn_perm(f2u(v[3]), tag, 0x07060503u);
  return r;
}
__device__ __forceinline__ u32 miss(u32x4 v, u32 tag) {
  const u32 a = __builtin_amdgcn_perm(v[1], v[0], 0x0c0c0400u), b = __builtin_amdgcn_perm(v[3], v[2], 0x04000c0cu);
  return (a | b) ^ tag;
}
__device__ __forceinline__ float val(u32 u) { return u2f((u & 0xffffff00u) | 0x80u); }
__device__ __forceinline__ void tr_pair(const u32x4& r0, const u32x4& r1, u32x4 (&f)[8], int k) {
#pragma unroll
  for (int d = 0; d < 4; ++d) { f[2 * d][k] = __builtin_amdgcn_perm(r1[d], r0[d], 0x05040100u); f[2 * d + 1][k] = __builtin_amdgcn_perm(r1[d], r0[d], 0x07060302u); }
}
__device__ __forceinline__ u64 rt() { u64 t; asm volatile("s_memrealtime %0\n\ts_waitcnt lgkmcnt(0)" : "=s"(t) :: "memory"); return t; }
#define STAMP(i) do { SB(); st[i] = rt(); SB(); } while (0)

constexpr int L_QP = 0;
constexpr int L_QF = 4096;
constexpr int L_TAG = 5120;
constexpr int L_ML = 5248;
constexpr int L_OP = 5632;
constexpr int L_YP = L_OP + 8192;
constexpr int L_END = L_YP + 4096;

__global__ void __launch_bounds__(320, 2) fused_decode(Params p) {
  __shared__ __attribute__((aligned(16))) unsigned char smem[L_END];
  LAS unsigned char* L = (LAS unsigned char*)smem;
  u64 st[12]; for (int i = 0; i < 12; ++i) st[i] = 0;
  STAMP(0);
  const u32 c = blockIdx.x, idx = c >> 3, grp = (c & 7) * 2 + (idx >> 4), j = idx & 15;
  const u32 br = grp >> 3, kvh = grp & 7;
  const u32 gq = grp ^ XQ, kq = gq & 7, go = grp ^ XO, ko = go & 7;
  const u32x16 ka = *(const CAS u32x16*)((const CAS char*)__builtin_amdgcn_kernarg_segment_ptr() + br * 64);
  const u32 tid = threadIdx.x, l = tid & 63, w = __builtin_amdgcn_readfirstlane(tid >> 6);
  const u32 g = l >> 4, i16 = l & 15;
  const u32 hh = j >> 2, cb = j & 3;
  constexpr float C2 = 0.08838834764831845f * 1.4426950408889634f;
  const f32x4 z = {0.f, 0.f, 0.f, 0.f};

  if (__builtin_expect(w < 4, 1)) {
    const u32 qc = j & 7, qh = j >> 3, b = j >> 3, s = j & 7;
    int oq = (qh * 256 + 64 * w + 8 * (l >> 3)) * 8192 + (kq * 512 + qc * 64) * 2 + (l & 7) * 16;
    int ox = ((i16 >> 3) * 512 + qh * 256 + 64 * w + 8 * (2 * g + ((i16 >> 2) & 1))) * 2;
    PIN(oq); PIN(ox); SB();
    const rsrc_t rq = mk(ka[0], ka[1], 512 * 4096 * 2), rx = mk(ka[2], ka[3], 2048), rk = mk(ka[4], ka[5], 2 * 8 * SK * 256), rv = mk(ka[6], ka[7], 2 * 8 * SK * 256),
                 ro = mk(ka[8], ka[9], 4096 * 512 * 2), rws = mk(ka[10], ka[11], WS_END), rout = mk(ka[12], ka[13], 4096);
    u32x4 X, Wq[8], K[16], V[16], Wo[8], F[8];
#pragma unroll
    for (int t = 0; t < 8; ++t) Wq[t] = ld16<AUX_NT>(rq, oq, t * 8192);
    X = ld16<0>(rx, ox, 0);
    SB();
    STAMP(1);
    const int kv0 = ((b * 8 + kvh) * SK + (SK - WND) + s * 256) * 256 + w * 1024;
    int ok = kv0 + ((l >> 2) & 3) * 256 + g * 64 + (l & 3) * 16;
    int ov = kv0 + l * 16;
    int oo = (ko * 512 + hh * 128 + 32 * w + 2 * g) * 1024 + cb * 256 + i16 * 16;
    const u32 prod = ((l >> 2) & 1) * 8 + (l >> 3);
    int po = WS_O + (go * 16 + prod) * 2048 + (hh * 16 + 4 * w + (l & 3)) * 16;
    int pm = WS_ML + (go * 16 + prod) * 128 + hh * 16;
    int lq = L_QP + w * 1024 + (g * 8 + (l & 7)) * 16;
    int ly = L_YP + w * 1024 + ((g & 1) * 32 + i16 * 2 + (g >> 1)) * 16;
    PIN(ok); PIN(ov); PIN(oo); PIN(po); PIN(pm); PIN(lq); PIN(ly);
#pragma unroll
    for (int t = 0; t < 8; ++t) { VMW(8); K[t] = ld16<AUX_NT>(rk, ok, t * 4096); SB(); if (t & 1) tr_pair(Wq[t - 1], Wq[t], F, t >> 1); }
    SB();
    K[8] = ld16<AUX_NT>(rk, ok, 8 * 4096);
    SB();
    STAMP(2);
    {
      f32x4 d[8];
#pragma unroll
      for (int e = 0; e < 8; ++e) d[e] = mfma(X, F[e], z);
      SB();
      if ((g & 1) == ((l >> 3) & 1)) {
        *(LAS f32x4*)(L + lq) = f32x4{d[0][0], d[1][0], d[2][0], d[3][0]};
        *(LAS f32x4*)(L + lq + 512) = f32x4{d[4][0], d[5][0], d[6][0], d[7][0]};
      }
    }
    BAR();
#pragma unroll
    for (int t = 9; t < 16; ++t) { VMW(7); K[t] = ld16<AUX_NT>(rk, ok, t * 4096); }
#pragma unroll
    for (int t = 0; t <= QPOS; ++t) { if (t < QPOS - 1) VMW(7); V[t] = ld16<AUX_NT>(rv, ov, t * 4096); }
    STAMP(3);
    BAR();
    STAMP(4);
    const u32x4 Q = *(const LAS u32x4*)(L + L_QF + l * 16);
    const u32 tag = *(const LAS u32*)(L + L_TAG);
    float sc[16];
    {
      f32x4 d[16];
#pragma unroll
      for (int t = 0; t < 16; ++t) d[t] = mfma(K[t], Q, z);
      SB();
#pragma unroll
      for (int t = 0; t < 16; ++t) sc[t] = d[t][0] + dpp<0x55>(d[t][1]);
      SB();
#pragma unroll
      for (int t = 0; t < 16; ++t) sc[t] += dpp<0xAA>(d[t][2]);
      SB();
#pragma unroll
      for (int t = 0; t < 16; ++t) sc[t] += dpp<0xFF>(d[t][3]);
      SB();
    }
    float m = sc[0];
#pragma unroll
    for (int t = 1; t < 16; ++t) m = fmaxf(m, sc[t]);
    m = x32_max(x16_max(m)) * C2;
    float lsum = 0.f;
#pragma unroll
    for (int t = 0; t < 16; ++t) { sc[t] = __builtin_amdgcn_exp2f(fmaf(sc[t], C2, -m)); lsum += sc[t]; }
    lsum = t16_sum(lsum, lsum); lsum = t32_sum(lsum, lsum);
    if (l < 16 && (l & 3) == 0) *(LAS f32x2*)(L + L_ML + (w * 4 + (l >> 2)) * 8) = f32x2{m, lsum};
    u32x4 P0, P1;
#pragma unroll
    for (int k = 0; k < 4; ++k) { P0[k] = pkbf(sc[2 * k], sc[2 * k + 1]); P1[k] = pkbf(sc[8 + 2 * k], sc[9 + 2 * k]); }
    BAR();
    STAMP(5);
#pragma unroll
    for (int t = QPOS + 1; t < 16; ++t) { VMW(7); V[t] = ld16<AUX_NT>(rv, ov, t * 4096); }
    f32x4 acc[8];
    VMW(7); Wo[0] = ld16<AUX_NT>(ro, oo, 0);
    VMW(7); Wo[1] = ld16<AUX_NT>(ro, oo + 1024, 0); SB();
#pragma unroll
    for (int k = 0; k < 4; ++k) tr_pair(V[2 * k], V[2 * k + 1], F, k);
#pragma unroll
    for (int e = 0; e < 8; ++e) acc[e] = mfma(P0, F[e], z);
    SB();
    tr_pair(V[8], V[9], F, 0);
#pragma unroll
    for (int t = 2; t < 8; ++t) { VMW(7); Wo[t] = ld16<AUX_NT>(ro, oo + (t & 1) * 1024, (t >> 1) * 8192); SB(); if (t & 1) tr_pair(V[7 + t], V[8 + t], F, t >> 1); }
    STAMP(6);
#pragma unroll
    for (int e = 0; e < 8; ++e) acc[e] = mfma(P1, F[e], acc[e]);
    SB();
    *(LAS f32x4*)(L + L_OP + w * 2048 + l * 16) = f32x4{acc[0][0], acc[1][0], acc[2][0], acc[3][0]};
    *(LAS f32x4*)(L + L_OP + w * 2048 + 1024 + l * 16) = f32x4{acc[4][0], acc[5][0], acc[6][0], acc[7][0]};
    BAR();
    u32x4 ml = ld16<AUX_SC1>(rws, pm, 0);
    SB();
#pragma unroll
    for (int k = 0; k < 4; ++k) tr_pair(Wo[2 * k], Wo[2 * k + 1], F, k);
#pragma unroll
    for (int e = 0; e < 8; ++e) PIN(F[e]);
    SB();
    if (OLEAD) __builtin_amdgcn_s_sleep(OLEAD);
    st[1] = st[0];
    u32x4 o0 = ld16<AUX_SC1>(rws, po, 0), o1 = ld16<AUX_SC1>(rws, po + 1024, 0);
    SB();
    STAMP(7);
#pragma unroll 1
    for (u32 spins = 0; spins < SPIN_CAP; ++spins) {
      const bool ms_ = miss(ml, tag) != 0;
      if (__builtin_amdgcn_ballot_w64(ms_) == 0) break;
      asm volatile("" ::: "memory");
      if (OSLEEP) __builtin_amdgcn_s_sleep(OSLEEP);
      if (!STICKY || ms_) ml = ld16<AUX_SC1>(rws, pm, 0);
    }
    float f;
    {
      const float ms = u2f(ml[0] & 0xffffff00u) + val(ml[1]), ls = val(ml[2]);
      float M = fmaxf(ms, dpp<0x128>(ms)); M = x32_max(x16_max(M));
      const float wt = __builtin_amdgcn_exp2f(ms - M);
      float Ls = wt * ls; Ls += dpp<0x128>(Ls); Ls = t16_sum(Ls, Ls); Ls = t32_sum(Ls, Ls);
      f = wt * __builtin_amdgcn_rcpf(Ls);
    }
    PIN(f);
    STAMP(2);
#pragma unroll 1
    for (u32 spins = 0; spins < SPIN_CAP; ++spins) {
      const bool ms_ = (miss(o0, tag) | miss(o1, tag)) != 0;
      if (__builtin_amdgcn_ballot_w64(ms_) == 0) break;
      asm volatile("" ::: "memory");
      if (OSLEEP) __builtin_amdgcn_s_sleep(OSLEEP);
      if (!STICKY || ms_) { o0 = ld16<AUX_SC1>(rws, po, 0); o1 = ld16<AUX_SC1>(rws, po + 1024, 0); }
      st[1] += 10;
    }
    STAMP(8);
    {
      float a[4];
#pragma unroll
      for (int e = 0; e < 4; ++e) a[e] = t32_sum(val(o0[e]) * f, val(o1[e]) * f);
      float b0 = t16_sum(a[0], a[2]), b1 = t16_sum(a[1], a[3]);
      b0 += dpp<0x128>(b0); b1 += dpp<0x128>(b1);
      const u32 pk = pkbf(b0, b1);
      const u32x4 A = {pk, dppu<0x55>(pk), dppu<0xAA>(pk), dppu<0xFF>(pk)};
      f32x4 d[8];
#pragma unroll
      for (int e = 0; e < 8; ++e) d[e] = mfma(A, F[e], z);
      SB();
      f32x4 y;
#pragma unroll
      for (int e = 0; e < 4; ++e) y[e] = (g >> 1) ? d[e + 4][0] : d[e][0];
      *(LAS f32x4*)(L + ly) = y;
    }
    BAR();
    STAMP(9);
    if (w == 0 && hh == 0) {
      const int py = WS_Y + (((br * 4 + cb) * 32 + (l >> 3)) * 64 + kvh * 8 + (l & 7)) * 16;
      u32x4 y0, y1, y2, y3;
#pragma unroll 1
      for (u32 spins = 0; spins < SPIN_CAP; ++spins) {
        y0 = ld16<AUX_SC1>(rws, py, 0); y1 = ld16<AUX_SC1>(rws, py, 8192); y2 = ld16<AUX_SC1>(rws, py, 16384); y3 = ld16<AUX_SC1>(rws, py, 24576);
        if (__builtin_amdgcn_ballot_w64((miss(y0, tag) | miss(y1, tag) | miss(y2, tag) | miss(y3, tag)) != 0) == 0) break;
        asm volatile("" ::: "memory");
      }
      STAMP(10);
      float y[4];
#pragma unroll
      for (int e = 0; e < 4; ++e) y[e] = (val(y0[e]) + val(y1[e])) + (val(y2[e]) + val(y3[e]));
      float b0 = t32_sum(y[0], y[2]), b1 = t32_sum(y[1], y[3]);
      b0 = t16_sum(b0, b0); b1 = t16_sum(b1, b1);
      b0 += dpp<0x128>(b0); b1 += dpp<0x128>(b1);
      const u32 u = kvh * 8 + (l & 7);
      if ((l & 24) == 0) raw_st32(pkbf(b0, b1), rout, ((u >> 5) * 1024 + br * 512 + cb * 128 + 4 * (u & 31) + 2 * (l >> 5)) * 2, 0, 0);
      if (kvh == 0 && cb == 0 && l == 0) raw_st32(tag, rws, WS_EPOCH + br * 256, 0, AUX_SC1);
    }
  } else {
    const u32 qc = j & 7, qh = j >> 3, b = j >> 3;
    const rsrc_t rws = mk(ka[10], ka[11], WS_END);
    const u32 ep = raw_ld32(rws, WS_EPOCH + br * 256, 0, AUX_SC1);
    int lqr = L_QP + ((l >> 3) & 1) * 512 + ((((l >> 4) & 1) * 2 + (l >> 5)) * 8 + (l & 7)) * 16;
    int sq = WS_Q + ((gq * 2 + ((l >> 4) & 1)) * 2 + qh) * 2048 + ((l >> 3) & 1) * 1024 + (qc * 8 + (l & 7)) * 16;
    int pq = WS_Q + (grp * 2 + b) * 4096 + ((i16 >> 2) * 16 + 4 * g + (l & 3)) * 16;
    int sm = WS_ML + (grp * 16 + j) * 128 + l * 16;
    int so = WS_O + (grp * 16 + j) * 2048 + l * 16;
    int sy = WS_Y + (((br * 4 + cb) * 32 + kvh * 4 + hh) * 64 + l) * 16;
    PIN(lqr); PIN(sq); PIN(pq); PIN(sm); PIN(so); PIN(sy);
    const u32 tag = __builtin_amdgcn_readfirstlane(((ep & 0x7f7f7f7fu) + 0x01010101u) ^ (ep & 0x80808080u));
    if (l == 0) *(LAS u32*)(L + L_TAG) = tag;
    STAMP(1);
    BAR();
    STAMP(2);
    {
      f32x4 sum = *(const LAS f32x4*)(L + lqr);
#pragma unroll
      for (int ww = 1; ww < 4; ++ww) sum += *(const LAS f32x4*)(L + lqr + ww * 1024);
#pragma unroll
      for (int e = 0; e < 4; ++e) sum[e] = t32_sum(sum[e], sum[e]);
      if (l < 32) raw_st128(tag4(sum, tag), rws, sq, 0, AUX_SC1);
    }
    STAMP(3);
    if (QSLEEP) __builtin_amdgcn_s_sleep(QSLEEP);
    {
      u32x4 a0, a1, b0, b1;
#pragma unroll 1
      for (u32 spins = 0; spins < SPIN_CAP; ++spins) {
        a0 = ld16<AUX_SC1>(rws, pq, 0); a1 = ld16<AUX_SC1>(rws, pq + 1024, 0); b0 = ld16<AUX_SC1>(rws, pq + 2048, 0); b1 = ld16<AUX_SC1>(rws, pq + 3072, 0);
        if (__builtin_amdgcn_ballot_w64((miss(a0, tag) | miss(a1, tag) | miss(b0, tag) | miss(b1, tag)) != 0) == 0) break;
        asm volatile("" ::: "memory");
        st[10] += 1;
      }
      u32x4 Q;
      Q[0] = pkbf(val(a0[0]) + val(b0[0]), val(a0[1]) + val(b0[1])); Q[1] = pkbf(val(a0[2]) + val(b0[2]), val(a0[3]) + val(b0[3]));
      Q[2] = pkbf(val(a1[0]) + val(b1[0]), val(a1[1]) + val(b1[1])); Q[3] = pkbf(val(a1[2]) + val(b1[2]), val(a1[3]) + val(b1[3]));
      *(LAS u32x4*)(L + L_QF + l * 16) = Q;
    }
    STAMP(4);
    BAR();
    BAR();
    STAMP(5);
    float fw[4];
    {
      f32x2 ml[4]; float mg[4];
#pragma unroll
      for (int ww = 0; ww < 4; ++ww) { ml[ww] = *(const LAS f32x2*)(L + L_ML + (ww * 4 + (l & 3)) * 8); mg[ww] = *(const LAS float*)(L + L_ML + (ww * 4 + g) * 8); }
      const float M = fmaxf(fmaxf(ml[0][0], ml[1][0]), fmaxf(ml[2][0], ml[3][0]));
      float Ls = 0.f;
#pragma unroll
      for (int ww = 0; ww < 4; ++ww) Ls = fmaf(__builtin_amdgcn_exp2f(ml[ww][0] - M), ml[ww][1], Ls);
      const float Mh = u2f(f2u(M) & 0xffffff00u);
      const f32x4 rec = {Mh, M - Mh, Ls, 0.f};
      if (l < 8) raw_st128(tag4(rec, tag), rws, sm, 0, AUX_SC1);
      const float Mg = fmaxf(fmaxf(mg[0], mg[1]), fmaxf(mg[2], mg[3]));
#pragma unroll
      for (int ww = 0; ww < 4; ++ww) { fw[ww] = __builtin_amdgcn_exp2f(mg[ww] - Mg); PIN(fw[ww]); }
    }
    BAR();
    STAMP(6);
    {
      f32x4 lo[4], hi[4];
#pragma unroll
      for (int ww = 0; ww < 4; ++ww) lo[ww] = *(const LAS f32x4*)(L + L_OP + ww * 2048 + l * 16);
#pragma unroll
      for (int ww = 0; ww < 4; ++ww) hi[ww] = *(const LAS f32x4*)(L + L_OP + ww * 2048 + 1024 + l * 16);
      SB();
      raw_st128(tag4(fw[0] * lo[0] + fw[1] * lo[1] + fw[2] * lo[2] + fw[3] * lo[3], tag), rws, so, 0, AUX_SC1);
      SB();
      raw_st128(tag4(fw[0] * hi[0] + fw[1] * hi[1] + fw[2] * hi[2] + fw[3] * hi[3], tag), rws, so + 1024, 0, AUX_SC1);
    }
    STAMP(7);
    BAR();
    STAMP(8);
    {
      f32x4 y = *(const LAS f32x4*)(L + L_YP + l * 16);
#pragma unroll
      for (int ww = 1; ww < 4; ++ww) y += *(const LAS f32x4*)(L + L_YP + ww * 1024 + l * 16);
      raw_st128(tag4(y, tag), rws, sy, 0, AUX_SC1);
    }
    STAMP(9);
    st[10] = st[0] + st[10] * 100;
  }
  STAMP(11);
  if (l == 0) { GAS u64* o = (GAS u64*)(((u64)ka[11] << 32 | ka[10]) + WS_STAMP) + (c * 5 + w) * 12; for (int i = 0; i < 12; ++i) o[i] = st[i]; }
}

#define NWAVES 5
__global__ void __launch_bounds__(256) k_enc(const u64* stp) {
  __shared__ u64 red[4];
  u64 t0 = rt();
  const int t = threadIdx.x;
  if (t == 0) { red[0] = ~0ull; red[1] = 0; red[2] = 0; red[3] = ~0ull; }
  __syncthreads();
  u64 s0 = ~0ull, v = 0;
  for (int w = 0; w < NWAVES; ++w) { u64 a = stp[(t * NWAVES + w) * 12]; s0 = a < s0 ? a : s0;
    if (KIND == 2 || (KIND == 0 && w < 4) || (KIND == 1 && w >= 4)) { u64 b = stp[(t * NWAVES + w) * 12 + IDX]; v = b > v ? b : v; } }
  atomicMin(&red[0], s0);
  __syncthreads();
  if (v) { atomicMax(&red[1], v - red[0]); atomicAdd(&red[2], v - red[0]); atomicMin(&red[3], v - red[0]); }
  __syncthreads();
  u64 m = STAT == 1 ? red[1] : STAT == 2 ? red[2] / 256 : STAT == 3 ? red[3] : 0;
  if (m > 5000) m = 5000;
  while (rt() - t0 < 1000 + m * 20) {}
}
extern "C" void kernel_launch(void* const* d_in, const int* in_sizes, int n_in, void* d_out, int out_size, void* d_ws, size_t ws_size, hipStream_t stream) {
  Params p;
  for (int i = 0; i < 2; ++i) {
    p.br[i].x = d_in[5 * i + 0]; p.br[i].k = d_in[5 * i + 1]; p.br[i].v = d_in[5 * i + 2]; p.br[i].wq = d_in[5 * i + 3]; p.br[i].wo = d_in[5 * i + 4];
    p.br[i].ws = d_ws; p.br[i].out = d_out; p.br[i].ws2 = d_ws;
  }
  fused_decode<<<256, 320, 0, stream>>>(p);
  k_enc<<<1, 256, 0, stream>>>((const u64*)((char*)d_ws + WS_STAMP));
}
```

```cpp
#define STAMPS 1
#define MODE 0
#include <hip/hip_runtime.h>
#include <hip/hip_bf16.h>
#include <stdint.h>

typedef unsigned int u32;
typedef unsigned long long u64;
typedef u32 u32x4 __attribute__((ext_vector_type(4)));
typedef u32 u32x2 __attribute__((ext_vector_type(2)));
typedef u32 u32x16 __attribute__((ext_vector_type(16)));
typedef float f32x4 __attribute__((ext_vector_type(4)));
typedef short bf16x8 __attribute__((ext_vector_type(8)));
#define LAS __attribute__((address_space(3)))
#define CAS __attribute__((address_space(4)))
#define GAS __attribute__((address_space(1)))

#define QSLEEP 24
#define QPOS 15
#define STICKY 1
#define OSLEEP 0
#define OLEAD 0
#define XQ 2
#define XO 2
#define PREF 1

constexpr int SK = 8192, WND = 2048;
constexpr int AUX_NT = 2, AUX_SC1 = 16;
constexpr int WS_EPOCH = 0;
constexpr int WS_Q = 4096;
constexpr int WS_ML = WS_Q + 131072;
constexpr int WS_O = WS_ML + 32768;
constexpr int WS_Y = WS_O + 524288;
constexpr int WS_END = WS_Y + 262144;
constexpr int WS_STAMP = 4 << 20;
constexpr u32 SPIN_CAP = 1u << 18;

struct Branch { const void* wq; const void* x; const void* k; const void* v; const void* wo; void* ws; void* out; void* ws2; };
struct Params { Branch br[2]; };

__device__ u32x4 raw_ld128(u32x4 rsrc, int voff, int soff, int aux) __asm("llvm.amdgcn.raw.buffer.load.v4i32");
__device__ u32 raw_ld32(u32x4 rsrc, int voff, int soff, int aux) __asm("llvm.amdgcn.raw.buffer.load.i32");
__device__ void raw_st128(u32x4 v, u32x4 rsrc, int voff, int soff, int aux) __asm("llvm.amdgcn.raw.buffer.store.v4i32");
__device__ void raw_st64(u32x2 v, u32x4 rsrc, int voff, int soff, int aux) __asm("llvm.amdgcn.raw.buffer.store.v2i32");
__device__ void raw_st32(u32 v, u32x4 rsrc, int voff, int soff, int aux) __asm("llvm.amdgcn.raw.buffer.store.i32");
typedef u32x4 rsrc_t;
__device__ __forceinline__ rsrc_t mk(u32 lo, u32 hi, u32 bytes) { return rsrc_t{lo, hi, bytes, 0x00020000u}; }
template <int AUX> __device__ __forceinline__ u32x4 ld16(rsrc_t r, int voff, int soff) { return raw_ld128(r, voff, soff, AUX); }
__device__ __forceinline__ f32x4 mfma(u32x4 a, u32x4 b, f32x4 c) {
  return __builtin_amdgcn_mfma_f32_16x16x32_bf16(__builtin_bit_cast(bf16x8, a), __builtin_bit_cast(bf16x8, b), c, 0, 0, 0);
}
__device__ __forceinline__ u32 f2u(float f) { return __builtin_bit_cast(u32, f); }
__device__ __forceinline__ float u2f(u32 u) { return __builtin_bit_cast(float, u); }
template <int CTRL> __device__ __forceinline__ u32 dppu(u32 x) { return (u32)__builtin_amdgcn_mov_dpp((int)x, CTRL, 0xf, 0xf, true); }
template <int CTRL> __device__ __forceinline__ float dpp(float x) { return u2f(dppu<CTRL>(f2u(x))); }
typedef __bf16 bf16x2_t __attribute__((ext_vector_type(2)));
typedef float f32x2 __attribute__((ext_vector_type(2)));
__device__ __forceinline__ u32 pkbf(float lo, float hi) { f32x2 v = {lo, hi}; return __builtin_bit_cast(u32, __builtin_convertvector(v, bf16x2_t)); }
__device__ __forceinline__ float x16_max(float x) { auto s = __builtin_amdgcn_permlane16_swap(f2u(x), f2u(x), false, false); return fmaxf(u2f(s[0]), u2f(s[1])); }
__device__ __forceinline__ float x32_max(float x) { auto s = __builtin_amdgcn_permlane32_swap(f2u(x), f2u(x), false, false); return fmaxf(u2f(s[0]), u2f(s[1])); }
__device__ __forceinline__ float t16_sum(float a, float b) { auto s = __builtin_amdgcn_permlane16_swap(f2u(a), f2u(b), false, false); return u2f(s[0]) + u2f(s[1]); }
__device__ __forceinline__ float t32_sum(float a, float b) { auto s = __builtin_amdgcn_permlane32_swap(f2u(a), f2u(b), false, false); return u2f(s[0]) + u2f(s[1]); }
#define SB() __builtin_amdgcn_sched_barrier(0)
#define STR2(x) #x
#define STR(x) STR2(x)
#define VMW(n) do { SB(); asm volatile("s_waitcnt vmcnt(" STR(n) ")" ::: "memory"); SB(); } while (0)
#define BAR() do { SB(); asm volatile("s_waitcnt lgkmcnt(0)\n\ts_barrier" ::: "memory"); SB(); } while (0)
#define PIN(x) asm volatile("" : "+v"(x))

__device__ __forceinline__ u32x4 tag4(f32x4 v, u32 tag) {
  u32x4 r;
  r[0] = __builtin_amdgcn_perm(f2u(v[0]), tag, 0x07060500u); r[1] = __builtin_amdgcn_perm(f2u(v[1]), tag, 0x07060501u);
  r[2] = __builtin_amdgcn_perm(f2u(v[2]), tag, 0x07060502u); r[3] = __builtin_amdgcn_perm(f2u(v[3]), tag, 0x07060503u);
  return r;
}
__device__ __forceinline__ u32 miss(u32x4 v, u32 tag) {
  const u32 a = __builtin_amdgcn_perm(v[1], v[0], 0x0c0c0400u), b = __builtin_amdgcn_perm(v[3], v[2], 0x04000c0cu);
  return (a | b) ^ tag;
}
__device__ __forceinline__ float val(u32 u) { return u2f((u & 0xffffff00u) | 0x80u); }
__device__ __forceinline__ void tr_pair(const u32x4& r0, const u32x4& r1, u32x4 (&f)[8], int k) {
#pragma unroll
  for (int d = 0; d < 4; ++d) { f[2 * d][k] = __builtin_amdgcn_perm(r1[d], r0[d], 0x05040100u); f[2 * d + 1][k] = __builtin_amdgcn_perm(r1[d], r0[d], 0x07060302u); }
}
__device__ __forceinline__ u64 rt() { u64 t; asm volatile("s_memrealtime %0\n\ts_waitcnt lgkmcnt(0)" : "=s"(t) :: "memory"); return t; }
#define STAMP(i) do { SB(); st[i] = rt(); SB(); } while (0)

constexpr int L_QP = 0;
constexpr int L_QF = 4096;
constexpr int L_TAG = 5120;
constexpr int L_ML = 5248;
constexpr int L_OP = 5632;
constexpr int L_YP = L_OP + 8192;
constexpr int L_END = L_YP + 4096;

__global__ void __launch_bounds__(320, 2) fused_decode(Params p) {
  __shared__ __attribute__((aligned(16))) unsigned char smem[L_END];
  LAS unsigned char* L = (LAS unsigned char*)smem;
  u64 st[12]; for (int i = 0; i < 12; ++i) st[i] = 0;
  STAMP(0);
  const u32 c = blockIdx.x, idx = c >> 3, grp = (c & 7) * 2 + (idx >> 4), j = idx & 15;
  const u32 br = grp >> 3, kvh = grp & 7;
  const u32 gq = grp ^ XQ, kq = gq & 7, go = grp ^ XO, ko = go & 7;
  const u32x16 ka = *(const CAS u32x16*)((const CAS char*)__builtin_amdgcn_kernarg_segment_ptr() + br * 64);
  const u32 tid = threadIdx.x, l = tid & 63, w = __builtin_amdgcn_readfirstlane(tid >> 6);
  const u32 g = l >> 4, i16 = l & 15;
  const u32 hh = j >> 2, cb = j & 3;
  constexpr float C2 = 0.08838834764831845f * 1.4426950408889634f;
  const f32x4 z = {0.f, 0.f, 0.f, 0.f};

  if (__builtin_expect(w < 4, 1)) {
    const u32 qc = j & 7, qh = j >> 3, b = j >> 3, s = j & 7;
    int oq = (qh * 256 + 64 * w + 8 * (l >> 3)) * 8192 + (kq * 512 + qc * 64) * 2 + (l & 7) * 16;
    int ox = ((i16 >> 3) * 512 + qh * 256 + 64 * w + 8 * (2 * g + ((i16 >> 2) & 1))) * 2;
    PIN(oq); PIN(ox); SB();
    const rsrc_t rq = mk(ka[0], ka[1], 512 * 4096 * 2), rx = mk(ka[2], ka[3], 2048), rk = mk(ka[4], ka[5], 2 * 8 * SK * 256), rv = mk(ka[6], ka[7], 2 * 8 * SK * 256),
                 ro = mk(ka[8], ka[9], 4096 * 512 * 2), rws = mk(ka[10], ka[11], WS_END), rout = mk(ka[12], ka[13], 4096);
    u32x4 X, Wq[8], K[16], V[16], Wo[8], F[8];
#pragma unroll
    for (int t = 0; t < 8; ++t) Wq[t] = ld16<AUX_NT>(rq, oq, t * 8192);
    X = ld16<0>(rx, ox, 0);
    SB();
    STAMP(1);
    const int kv0 = ((b * 8 + kvh) * SK + (SK - WND) + s * 256) * 256 + w * 1024;
    int ok = kv0 + ((l >> 2) & 3) * 256 + g * 64 + (l & 3) * 16;
    int ov = kv0 + l * 16;
    int oo = (ko * 512 + hh * 128 + 32 * w + 2 * g) * 1024 + cb * 256 + i16 * 16;
    const u32 prod = ((l >> 2) & 1) * 8 + (l >> 3);
    int po = WS_O + (go * 16 + prod) * 2048 + (hh * 16 + 4 * w + (l & 3)) * 16;
    int pm = WS_ML + (go * 16 + prod) * 128 + hh * 16;
    int lq = L_QP + w * 1024 + (g * 8 + (l & 7)) * 16;
    int ly = L_YP + w * 1024 + ((g & 1) * 32 + i16 * 2 + (g >> 1)) * 16;
    PIN(ok); PIN(ov); PIN(oo); PIN(po); PIN(pm); PIN(lq); PIN(ly);
#pragma unroll
    for (int t = 0; t < 8; ++t) { VMW(8); K[t] = ld16<AUX_NT>(rk, ok, t * 4096); SB(); if (t & 1) tr_pair(Wq[t - 1], Wq[t], F, t >> 1); }
    SB();
    K[8] = ld16<AUX_NT>(rk, ok, 8 * 4096);
    SB();
    STAMP(2);
    {
      f32x4 d[8];
#pragma unroll
      for (int e = 0; e < 8; ++e) d[e] = mfma(X, F[e], z);
      SB();
      if ((g & 1) == ((l >> 3) & 1)) {
        *(LAS f32x4*)(L + lq) = f32x4{d[0][0], d[1][0], d[2][0], d[3][0]};
        *(LAS f32x4*)(L + lq + 512) = f32x4{d[4][0], d[5][0], d[6][0], d[7][0]};
      }
    }
    BAR();
#pragma unroll
    for (int t = 9; t < 16; ++t) { VMW(7); K[t] = ld16<AUX_NT>(rk, ok, t * 4096); }
#pragma unroll
    for (int t = 0; t <= QPOS; ++t) { if (t < QPOS - 1) VMW(7); V[t] = ld16<AUX_NT>(rv, ov, t * 4096); }
    STAMP(3);
    BAR();
    STAMP(4);
    const u32x4 Q = *(const LAS u32x4*)(L + L_QF + l * 16);
    const u32 tag = *(const LAS u32*)(L + L_TAG);
    float sc[16];
    {
      f32x4 d[16];
#pragma unroll
      for (int t = 0; t < 16; ++t) d[t] = mfma(K[t], Q, z);
      SB();
#pragma unroll
      for (int t = 0; t < 16; ++t) sc[t] = d[t][0] + dpp<0x55>(d[t][1]);
      SB();
#pragma unroll
      for (int t = 0; t < 16; ++t) sc[t] += dpp<0xAA>(d[t][2]);
      SB();
#pragma unroll
      for (int t = 0; t < 16; ++t) sc[t] += dpp<0xFF>(d[t][3]);
      SB();
    }
    float m = sc[0];
#pragma unroll
    for (int t = 1; t < 16; ++t) m = fmaxf(m, sc[t]);
    m = x32_max(x16_max(m)) * C2;
    float lsum = 0.f;
#pragma unroll
    for (int t = 0; t < 16; ++t) { sc[t] = __builtin_amdgcn_exp2f(fmaf(sc[t], C2, -m)); lsum += sc[t]; }
    lsum = t16_sum(lsum, lsum); lsum = t32_sum(lsum, lsum);
    if (l < 16 && (l & 3) == 0) *(LAS f32x2*)(L + L_ML + (w * 4 + (l >> 2)) * 8) = f32x2{m, lsum};
    u32x4 P0, P1;
#pragma unroll
    for (int k = 0; k < 4; ++k) { P0[k] = pkbf(sc[2 * k], sc[2 * k + 1]); P1[k] = pkbf(sc[8 + 2 * k], sc[9 + 2 * k]); }
    BAR();
    STAMP(5);
#pragma unroll
    for (int t = QPOS + 1; t < 16; ++t) { VMW(7); V[t] = ld16<AUX_NT>(rv, ov, t * 4096); }
    f32x4 acc[8];
    VMW(7); Wo[0] = ld16<AUX_NT>(ro, oo, 0);
    VMW(7); Wo[1] = ld16<AUX_NT>(ro, oo + 1024, 0); SB();
#pragma unroll
    for (int k = 0; k < 4; ++k) tr_pair(V[2 * k], V[2 * k + 1], F, k);
#pragma unroll
    for (int e = 0; e < 8; ++e) acc[e] = mfma(P0, F[e], z);
    SB();
    tr_pair(V[8], V[9], F, 0);
#pragma unroll
    for (int t = 2; t < 8; ++t) { VMW(7); Wo[t] = ld16<AUX_NT>(ro, oo + (t & 1) * 1024, (t >> 1) * 8192); SB(); if (t & 1) tr_pair(V[7 + t], V[8 + t], F, t >> 1); }
    STAMP(6);
#pragma unroll
    for (int e = 0; e < 8; ++e) acc[e] = mfma(P1, F[e], acc[e]);
    SB();
    *(LAS f32x4*)(L + L_OP + w * 2048 + l * 16) = f32x4{acc[0][0], acc[1][0], acc[2][0], acc[3][0]};
    *(LAS f32x4*)(L + L_OP + w * 2048 + 1024 + l * 16) = f32x4{acc[4][0], acc[5][0], acc[6][0], acc[7][0]};
    BAR();
    u32x4 ml = ld16<AUX_SC1>(rws, pm, 0);
    SB();
#pragma unroll
    for (int k = 0; k < 4; ++k) tr_pair(Wo[2 * k], Wo[2 * k + 1], F, k);
#pragma unroll
    for (int e = 0; e < 8; ++e) PIN(F[e]);
    SB();
    if (OLEAD) __builtin_amdgcn_s_sleep(OLEAD);
    st[1] = st[0];
    u32x4 o0 = ld16<AUX_SC1>(rws, po, 0), o1 = ld16<AUX_SC1>(rws, po + 1024, 0);
    SB();
    STAMP(7);
#pragma unroll 1
    for (u32 spins = 0; spins < SPIN_CAP; ++spins) {
      const bool ms_ = miss(ml, tag) != 0;
      if (__builtin_amdgcn_ballot_w64(ms_) == 0) break;
      asm volatile("" ::: "memory");
      if (OSLEEP) __builtin_amdgcn_s_sleep(OSLEEP);
      if (!STICKY || ms_) ml = ld16<AUX_SC1>(rws, pm, 0);
    }
    float f;
    {
      const float ms = u2f(ml[0] & 0xffffff00u) + val(ml[1]), ls = val(ml[2]);
      float M = fmaxf(ms, dpp<0x128>(ms)); M = x32_max(x16_max(M));
      const float wt = __builtin_amdgcn_exp2f(ms - M);
      float Ls = wt * ls; Ls += dpp<0x128>(Ls); Ls = t16_sum(Ls, Ls); Ls = t32_sum(Ls, Ls);
      f = wt * __builtin_amdgcn_rcpf(Ls);
    }
    PIN(f);
    STAMP(2);
#pragma unroll 1
    for (u32 spins = 0; spins < SPIN_CAP; ++spins) {
      const bool ms_ = (miss(o0, tag) | miss(o1, tag)) != 0;
      if (__builtin_amdgcn_ballot_w64(ms_) == 0) break;
      asm volatile("" ::: "memory");
      if (OSLEEP) __builtin_amdgcn_s_sleep(OSLEEP);
      if (!STICKY || ms_) { o0 = ld16<AUX_SC1>(rws, po, 0); o1 = ld16<AUX_SC1>(rws, po + 1024, 0); }
      st[1] += 10;
    }
    STAMP(8);
    {
      float a[4];
#pragma unroll
      for (int e = 0; e < 4; ++e) a[e] = t32_sum(val(o0[e]) * f, val(o1[e]) * f);
      float b0 = t16_sum(a[0], a[2]), b1 = t16_sum(a[1], a[3]);
      b0 += dpp<0x128>(b0); b1 += dpp<0x128>(b1);
      const u32 pk = pkbf(b0, b1);
      const u32x4 A = {pk, dppu<0x55>(pk), dppu<0xAA>(pk), dppu<0xFF>(pk)};
      f32x4 d[8];
#pragma unroll
      for (int e = 0; e < 8; ++e) d[e] = mfma(A, F[e], z);
      SB();
      f32x4 y;
#pragma unroll
      for (int e = 0; e < 4; ++e) y[e] = (g >> 1) ? d[e + 4][0] : d[e][0];
      *(LAS f32x4*)(L + ly) = y;
    }
    BAR();
    STAMP(9);
    if (w == 0 && hh == 0) {
      const int py = WS_Y + (((br * 4 + cb) * 32 + (l >> 3)) * 64 + kvh * 8 + (l & 7)) * 16;
      u32x4 y0, y1, y2, y3;
#pragma unroll 1
      for (u32 spins = 0; spins < SPIN_CAP; ++spins) {
        y0 = ld16<AUX_SC1>(rws, py, 0); y1 = ld16<AUX_SC1>(rws, py, 8192); y2 = ld16<AUX_SC1>(rws, py, 16384); y3 = ld16<AUX_SC1>(rws, py, 24576);
        if (__builtin_amdgcn_ballot_w64((miss(y0, tag) | miss(y1, tag) | miss(y2, tag) | miss(y3, tag)) != 0) == 0) break;
        asm volatile("" ::: "memory");
      }
      STAMP(10);
      float y[4];
#pragma unroll
      for (int e = 0; e < 4; ++e) y[e] = (val(y0[e]) + val(y1[e])) + (val(y2[e]) + val(y3[e]));
      float b0 = t32_sum(y[0], y[2]), b1 = t32_sum(y[1], y[3]);
      b0 = t16_sum(b0, b0); b1 = t16_sum(b1, b1);
      b0 += dpp<0x128>(b0); b1 += dpp<0x128>(b1);
      const u32 u = kvh * 8 + (l & 7);
      if ((l & 24) == 0) raw_st32(pkbf(b0, b1), rout, ((u >> 5) * 1024 + br * 512 + cb * 128 + 4 * (u & 31) + 2 * (l >> 5)) * 2, 0, 0);
      if (kvh == 0 && cb == 0 && l == 0) raw_st32(tag, rws, WS_EPOCH + br * 256, 0, AUX_SC1);
    }
  } else {
    const u32 qc = j & 7, qh = j >> 3, b = j >> 3;
    const rsrc_t rws = mk(ka[10], ka[11], WS_END);
    const u32 ep = raw_ld32(rws, WS_EPOCH + br * 256, 0, AUX_SC1);
    int lqr = L_QP + ((l >> 3) & 1) * 512 + ((((l >> 4) & 1) * 2 + (l >> 5)) * 8 + (l & 7)) * 16;
    int sq = WS_Q + ((gq * 2 + ((l >> 4) & 1)) * 2 + qh) * 2048 + ((l >> 3) & 1) * 1024 + (qc * 8 + (l & 7)) * 16;
    int pq = WS_Q + (grp * 2 + b) * 4096 + ((i16 >> 2) * 16 + 4 * g + (l & 3)) * 16;
    int sm = WS_ML + (grp * 16 + j) * 128 + l * 16;
    int so = WS_O + (grp * 16 + j) * 2048 + l * 16;
    int sy = WS_Y + (((br * 4 + cb) * 32 + kvh * 4 + hh) * 64 + l) * 16;
    PIN(lqr); PIN(sq); PIN(pq); PIN(sm); PIN(so); PIN(sy);
    const u32 tag = __builtin_amdgcn_readfirstlane(((ep & 0x7f7f7f7fu) + 0x01010101u) ^ (ep & 0x80808080u));
    if (l == 0) *(LAS u32*)(L + L_TAG) = tag;
    STAMP(1);
    BAR();
    STAMP(2);
    {
      f32x4 sum = *(const LAS f32x4*)(L + lqr);
#pragma unroll
      for (int ww = 1; ww < 4; ++ww) sum += *(const LAS f32x4*)(L + lqr + ww * 1024);
#pragma unroll
      for (int e = 0; e < 4; ++e) sum[e] = t32_sum(sum[e], sum[e]);
      if (l < 32) raw_st128(tag4(sum, tag), rws, sq, 0, AUX_SC1);
    }
    STAMP(3);
    if (QSLEEP) __builtin_amdgcn_s_sleep(QSLEEP);
    {
      u32x4 a0, a1, b0, b1;
#pragma unroll 1
      for (u32 spins = 0; spins < SPIN_CAP; ++spins) {
        a0 = ld16<AUX_SC1>(rws, pq, 0); a1 = ld16<AUX_SC1>(rws, pq + 1024, 0); b0 = ld16<AUX_SC1>(rws, pq + 2048, 0); b1 = ld16<AUX_SC1>(rws, pq + 3072, 0);
        if (__builtin_amdgcn_ballot_w64((miss(a0, tag) | miss(a1, tag) | miss(b0, tag) | miss(b1, tag)) != 0) == 0) break;
        asm volatile("" ::: "memory");
        st[10] += 1;
      }
      u32x4 Q;
      Q[0] = pkbf(val(a0[0]) + val(b0[0]), val(a0[1]) + val(b0[1])); Q[1] = pkbf(val(a0[2]) + val(b0[2]), val(a0[3]) + val(b0[3]));
      Q[2] = pkbf(val(a1[0]) + val(b1[0]), val(a1[1]) + val(b1[1])); Q[3] = pkbf(val(a1[2]) + val(b1[2]), val(a1[3]) + val(b1[3]));
      *(LAS u32x4*)(L + L_QF + l * 16) = Q;
    }
    STAMP(4);
    BAR();
    BAR();
    STAMP(5);
    float fw[4];
    {
      f32x2 ml[4]; float mg[4];
#pragma unroll
      for (int ww = 0; ww < 4; ++ww) { ml[ww] = *(const LAS f32x2*)(L + L_ML + (ww * 4 + (l & 3)) * 8); mg[ww] = *(const LAS float*)(L + L_ML + (ww * 4 + g) * 8); }
      const float M = fmaxf(fmaxf(ml[0][0], ml[1][0]), fmaxf(ml[2][0], ml[3][0]));
      float Ls = 0.f;
#pragma unroll
      for (int ww = 0; ww < 4; ++ww) Ls = fmaf(__builtin_amdgcn_exp2f(ml[ww][0] - M), ml[ww][1], Ls);
      const float Mh = u2f(f2u(M) & 0xffffff00u);
      const f32x4 rec = {Mh, M - Mh, Ls, 0.f};
      if (l < 8) raw_st128(tag4(rec, tag), rws, sm, 0, AUX_SC1);
      const float Mg = fmaxf(fmaxf(mg[0], mg[1]), fmaxf(mg[2], mg[3]));
#pragma unroll
      for (int ww = 0; ww < 4; ++ww) { fw[ww] = __builtin_amdgcn_exp2f(mg[ww] - Mg); PIN(fw[ww]); }
    }
    BAR();
    STAMP(6);
    {
      f32x4 lo[4], hi[4];
#pragma unroll
      for (int ww = 0; ww < 4; ++ww) lo[ww] = *(const LAS f32x4*)(L + L_OP + ww * 2048 + l * 16);
#pragma unroll
      for (int ww = 0; ww < 4; ++ww) hi[ww] = *(const LAS f32x4*)(L + L_OP + ww * 2048 + 1024 + l * 16);
      SB();
      raw_st128(tag4(fw[0] * lo[0] + fw[1] * lo[1] + fw[2] * lo[2] + fw[3] * lo[3], tag), rws, so, 0, AUX_SC1);
      SB();
      raw_st128(tag4(fw[0] * hi[0] + fw[1] * hi[1] + fw[2] * hi[2] + fw[3] * hi[3], tag), rws, so + 1024, 0, AUX_SC1);
    }
    STAMP(7);
    BAR();
    STAMP(8);
    {
      f32x4 y = *(const LAS f32x4*)(L + L_YP + l * 16);
#pragma unroll
      for (int ww = 1; ww < 4; ++ww) y += *(const LAS f32x4*)(L + L_YP + ww * 1024 + l * 16);
      raw_st128(tag4(y, tag), rws, sy, 0, AUX_SC1);
    }
    STAMP(9);
    st[10] = st[0] + st[10] * 100;
  }
  STAMP(11);
  if (l == 0) { GAS u64* o = (GAS u64*)(((u64)ka[11] << 32 | ka[10]) + WS_STAMP) + (c * 5 + w) * 12; for (int i = 0; i < 12; ++i) o[i] = st[i]; }
}

#define NWAVES 5
__global__ void k_pre(u64* o) { u64 t = rt(); if (threadIdx.x == 0) o[0] = t; }
__global__ void __launch_bounds__(256) k_enc(const u64* stp, const u64* pre) {
  __shared__ u64 red[4];
  u64 t0 = rt();
  const int t = threadIdx.x;
  if (t == 0) { red[0] = ~0ull; red[1] = 0; red[2] = ~0ull; }
  __syncthreads();
  u64 s0 = ~0ull, v = 0;
  for (int w = 0; w < NWAVES; ++w) { u64 a = stp[(t * NWAVES + w) * 12]; s0 = a < s0 ? a : s0; u64 b = stp[(t * NWAVES + w) * 12 + 11]; v = b > v ? b : v; }
  atomicMin(&red[0], s0); atomicMax(&red[1], v); atomicMin(&red[2], t0);
  __syncthreads();
  u64 m = MODE == 1 ? red[2] - red[1] : MODE == 2 ? red[0] - pre[0] : 0;
  if (m > 5000) m = 5000;
  while (rt() - t0 < 1000 + m * 20) {}
}
extern "C" void kernel_launch(void* const* d_in, const int* in_sizes, int n_in, void* d_out, int out_size, void* d_ws, size_t ws_size, hipStream_t stream) {
  Params p;
  for (int i = 0; i < 2; ++i) {
    p.br[i].x = d_in[5 * i + 0]; p.br[i].k = d_in[5 * i + 1]; p.br[i].v = d_in[5 * i + 2]; p.br[i].wq = d_in[5 * i + 3]; p.br[i].wo = d_in[5 * i + 4];
    p.br[i].ws = d_ws; p.br[i].out = d_out; p.br[i].ws2 = d_ws;
  }
  fused_decode<<<256, 320, 0, stream>>>(p);
  k_enc<<<1, 256, 0, stream>>>((const u64*)((char*)d_ws + WS_STAMP), (const u64*)((char*)d_ws + WS_STAMP + (1 << 20)));
}
```

```cpp
#include <hip/hip_runtime.h>
#include <stdint.h>

struct Branch {
  const void* wq; uint32_t wq_bytes; uint32_t cfg;
  const void* x; const void* k; const void* v; const void* wo; void* ws; void* out;
};
struct Params { Branch br[2]; };
static_assert(sizeof(Branch) == 64 && sizeof(Params) == 128, "one 64-byte record per branch, no padding");

__global__ void __launch_bounds__(320, 2) fused_decode(Params p) {
  __shared__ __attribute__((aligned(16))) unsigned char smem[17920];
  const uint64_t ka = (uint64_t)__builtin_amdgcn_kernarg_segment_ptr();
  const uint32_t wg = blockIdx.x;
  uint32_t tid = threadIdx.x;
  asm volatile(
      "s_bfe_u32 s3, s2, 0x10002\n"
      "s_lshl_b32 s4, s3, 6\n"
      "s_load_dwordx16 s[16:31], s[0:1], s4\n"
      "v_readfirstlane_b32 s5, v0\n"
      "s_lshr_b32 s6, s2, 3\n"
      "s_and_b32 s7, s2, 3\n"
      "s_and_b32 s8, s6, 15\n"
      "s_lshr_b32 s4, s6, 4\n"
      "s_lshl1_add_u32 s9, s7, s4\n"
      "s_cmpk_lt_u32 s5, 0x100\n"
      "s_cbranch_scc1 .Lfd_stream\n"
      "s_cmpk_ge_u32 s6, 2\n"
      "s_cbranch_scc1 .Lfd_comm\n"
      "s_getpc_b64 s[10:11]\n"
      ".Lfd_pc:\n"
      "v_and_b32 v1, 63, v0\n"
      "v_lshlrev_b32 v1, 7, v1\n"
      "v_min_u32 v2, .Lfd_end-.Lfd_pc-4, v1\n"
      "global_load_dword v60, v2, s[10:11]\n"
      "v_add_u32 v1, 0x2000, v1\n"
      "v_min_u32 v2, .Lfd_end-.Lfd_pc-4, v1\n"
      "global_load_dword v61, v2, s[10:11]\n"
      "s_branch .Lfd_comm\n"
      ".Lfd_stream:\n"
      "v_and_b32 v2, 0xf8, v0\n"
      "v_and_b32 v3, 7, v0\n"
      "v_lshlrev_b32 v15, 4, v3\n"
      "v_lshl_or_b32 v2, v2, 13, v15\n"
      "s_xor_b32 s4, s9, 2\n"
      "s_lshl_b32 s57, s4, 10\n"
      "s_lshr_b32 s10, s8, 3\n"
      "s_lshl_b32 s4, s10, 21\n"
      "s_or_b32 s57, s57, s4\n"
      "s_and_b32 s11, s8, 7\n"
      "s_lshl_b32 s4, s11, 7\n"
      "s_or_b32 s57, s57, s4\n"
      "s_waitcnt lgkmcnt(0)\n"
      "buffer_load_dwordx4 v[112:115], v2, s[16:19], s57 offen nt\n"
      "s_addk_i32 s57, 0x2000\n"
      "buffer_load_dwordx4 v[116:119], v2, s[16:19], s57 offen nt\n"
      "s_memrealtime vcc\n"
      "v_and_b32 v3, 0xf0, v0\n"
      "v_lshlrev_b32 v3, 1, v3\n"
      "v_and_b32 v148, 8, v0\n"
      "v_lshl_or_b32 v3, v148, 7, v3\n"
      "v_and_b32 v148, 4, v0\n"
      "v_lshl_or_b32 v3, v148, 2, v3\n"
      "s_mov_b64 s[40:41], s[20:21]\n"
      "s_movk_i32 s42, 0x800\n"
      "s_mov_b32 s43, s19\n"
      "s_lshr_b32 s4, s8, 3\n"
      "s_lshl_b32 s4, s4, 9\n"
      "buffer_load_dwordx4 v[144:147], v3, s[40:43], s4 offen\n"
      "s_waitcnt lgkmcnt(0)\n"
      "s_mov_b32 s56, vcc_lo\n"
      "s_mov_b64 s[36:37], s[22:23]\n"
      "s_brev_b32 s38, 64\n"
      "s_mov_b32 s39, s19\n"
      "s_lshl3_add_u32 s4, s10, s9\n"
      "s_lshl_b32 s4, s4, 21\n"
      "s_lshl_b32 s15, s11, 16\n"
      "s_or_b32 s4, s4, s15\n"
      "s_or_b32 s4, s4, 0x180000\n"
      "v_lshl_add_u32 v5, v0, 4, s4\n"
      "v_and_b32 v4, 0xc3, v0\n"
      "v_lshl_add_u32 v4, v4, 4, s4\n"
      "v_and_b32 v1, 12, v0\n"
      "v_lshl_or_b32 v4, v1, 6, v4\n"
      "v_and_b32 v1, 48, v0\n"
      "v_lshl_or_b32 v4, v1, 2, v4\n"
      "s_mov_b32 s59, 0xf000\n"
      "s_mov_b32 s69, 0x05040100\n"
      "s_mov_b32 s70, 0x07060302\n"
      "s_and_b32 s4, s11, 6\n"
      "s_add_u32 s4, s4, s9\n"
      "s_lshl_b32 s58, s4, 12\n"
      "s_and_b32 s58, s58, s59\n"
      ".Lfd_u1:\n"
      "s_memrealtime vcc\n"
      "s_waitcnt lgkmcnt(0)\n"
      "s_sub_u32 vcc_lo, vcc_lo, s56\n"
      "s_cmpk_lt_i32 vcc_lo, 14\n"
      "s_cbranch_scc1 .Lfd_u1\n"
      "s_addk_i32 s57, 0x2000\n"
      "buffer_load_dwordx4 v[120:123], v2, s[16:19], s57 offen nt\n"
      "s_lshr_b32 s4, s6, 4\n"
      "s_add_u32 s4, s4, 1\n"
      "s_xor_b32 s15, s4, s7\n"
      "s_bfe_u32 s4, s6, 0x10003\n"
      "s_lshl1_add_u32 s10, s15, s4\n"
      "s_bfe_u32 s11, s6, 0x20001\n"
      "s_and_b32 s12, s6, 1\n"
      "s_and_b32 s4, s7, 2\n"
      "s_or_b32 s12, s12, s4\n"
      "s_xor_b32 s13, s7, 2\n"
      "s_lshr_b32 s14, s6, 2\n"
      "s_and_b32 s74, s6, 3\n"
      "s_mov_b32 s46, 0x400000\n"
      "s_mov_b32 s47, s19\n"
      "s_movk_i32 s54, 0x1000\n"
      "s_mov_b32 s55, s19\n"
      "s_lshl_b32 s60, s10, 19\n"
      "s_lshl_b32 s4, s11, 17\n"
      "s_or_b32 s60, s60, s4\n"
      "s_lshl_b32 s4, s12, 8\n"
      "s_or_b32 s60, s60, s4\n"
      "s_add_u32 s48, s28, 0x1fffff\n"
      "s_addc_u32 s49, s29, 0\n"
      "s_and_b32 s48, s48, 0xffe00000\n"
      "s_lshl_b32 s50, s3, 20\n"
      "s_or_b32 s48, s48, s50\n"
      "s_mov_b32 s50, 983040\n"
      "s_mov_b32 s51, s19\n"
      ".Lfd_u2:\n"
      "s_memrealtime vcc\n"
      "s_waitcnt lgkmcnt(0)\n"
      "s_sub_u32 vcc_lo, vcc_lo, s56\n"
      "s_cmpk_lt_i32 vcc_lo, 28\n"
      "s_cbranch_scc1 .Lfd_u2\n"
      "s_addk_i32 s57, 0x2000\n"
      "buffer_load_dwordx4 v[124:127], v2, s[16:19], s57 offen nt\n"
      "s_lshl_b32 s61, s10, 16\n"
      "s_lshl_b32 s4, s11, 8\n"
      "s_or_b32 s61, s61, s4\n"
      "s_add_u32 s61, s61, 163840\n"
      "s_lshr_b32 s62, s10, 2\n"
      "s_lshl_b32 s62, s62, 14\n"
      "s_and_b32 s4, s10, 3\n"
      "s_lshl_b32 s4, s4, 11\n"
      "s_or_b32 s62, s62, s4\n"
      "s_lshl4_add_u32 s62, s11, s62\n"
      "s_add_u32 s62, s62, 131072\n"
      "s_lshl_b32 s63, s13, 16\n"
      "s_lshl_b32 s4, s14, 7\n"
      "s_or_b32 s63, s63, s4\n"
      "s_add_u32 s63, s63, 688128\n"
      "s_lshr_b32 s64, s14, 2\n"
      "s_lshl1_add_u32 s64, s64, s3\n"
      "s_lshl2_add_u32 s64, s64, s13\n"
      "s_and_b32 s4, s14, 3\n"
      "s_lshl2_add_u32 s64, s64, s4\n"
      "s_lshl_b32 s64, s64, 6\n"
      "s_xor_b32 s4, s5, 64\n"
      "s_or_b32 s74, s74, s4\n"
      "s_mov_b32 s71, 0x3e0293ee\n"
      "s_mov_b32 s72, 262144\n"
      "s_movk_i32 s79, 0xff\n"
      ".Lfd_u3:\n"
      "s_memrealtime vcc\n"
      "s_waitcnt lgkmcnt(0)\n"
      "s_sub_u32 vcc_lo, vcc_lo, s56\n"
      "s_cmpk_lt_i32 vcc_lo, 42\n"
      "s_cbranch_scc1 .Lfd_u3\n"
      "s_addk_i32 s57, 0x2000\n"
      "buffer_load_dwordx4 v[128:131], v2, s[16:19], s57 offen nt\n"
      "v_and_b32 v7, 63, v0\n"
      "v_lshlrev_b32 v1, 4, v7\n"
      "v_lshrrev_b32 v8, 2, v7\n"
      "v_lshlrev_b32 v8, 7, v8\n"
      "v_lshrrev_b32 v9, 3, v7\n"
      "v_lshl_or_b32 v9, v9, 10, v15\n"
      "v_lshrrev_b32 v10, 5, v7\n"
      "v_lshlrev_b32 v10, 2, v10\n"
      "v_lshrrev_b32 v14, 1, v15\n"
      "v_or_b32 v10, v10, v14\n"
      "v_and_b32 v6, 0xf0, v1\n"
      "v_and_b32 v14, 48, v0\n"
      "v_lshl_or_b32 v6, v14, 7, v6\n"
      "v_lshl_or_b32 v11, v14, 3, v15\n"
      "v_and_b32 v7, 48, v1\n"
      "v_lshl_or_b32 v7, v14, 10, v7\n"
      "v_and_b32 v14, 12, v0\n"
      "v_lshl_or_b32 v7, v14, 9, v7\n"
      "v_or_b32 v7, s5, v7\n"
      "s_lshl_b32 s4, s5, 9\n"
      "v_or_b32 v6, s4, v6\n"
      "s_lshl_b32 s4, s5, 4\n"
      "v_or_b32 v11, s4, v11\n"
      ".Lfd_u4:\n"
      "s_memrealtime vcc\n"
      "s_waitcnt lgkmcnt(0)\n"
      "s_sub_u32 vcc_lo, vcc_lo, s56\n"
      "s_cmpk_lt_i32 vcc_lo, 56\n"
      "s_cbranch_scc1 .Lfd_u4\n"
      "s_addk_i32 s57, 0x2000\n"
      "buffer_load_dwordx4 v[132:135], v2, s[16:19], s57 offen nt\n"
      "v_and_b32 v13, 0xf0, v1\n"
      "v_lshlrev_b32 v13, 1, v13\n"
      "v_and_b32 v14, 16, v0\n"
      "v_lshl_or_b32 v13, v14, 5, v13\n"
      "v_and_b32 v14, 32, v0\n"
      "v_lshrrev_b32 v14, 1, v14\n"
      "s_lshl_b32 s4, s5, 4\n"
      "v_or3_b32 v13, v13, v14, s4\n"
      "s_lshl_b32 s4, s5, 5\n"
      "v_or_b32 v12, s4, v1\n"
      "v_and_b32 v14, 12, v0\n"
      "v_lshlrev_b32 v14, 1, v14\n"
      "s_lshr_b32 s4, s5, 1\n"
      "v_or_b32 v14, s4, v14\n"
      "v_mov_b32 v15, 0\n"
      ".Lfd_u5:\n"
      "s_memrealtime vcc\n"
      "s_waitcnt lgkmcnt(0)\n"
      "s_sub_u32 vcc_lo, vcc_lo, s56\n"
      "s_cmpk_lt_i32 vcc_lo, 70\n"
      "s_cbranch_scc1 .Lfd_u5\n"
      "s_addk_i32 s57, 0x2000\n"
      "buffer_load_dwordx4 v[136:139], v2, s[16:19], s57 offen nt\n"
      ".Lfd_u6:\n"
      "s_memrealtime vcc\n"
      "s_waitcnt lgkmcnt(0)\n"
      "s_sub_u32 vcc_lo, vcc_lo, s56\n"
      "s_cmpk_lt_i32 vcc_lo, 84\n"
      "s_cbranch_scc1 .Lfd_u6\n"
      "s_addk_i32 s57, 0x2000\n"
      "buffer_load_dwordx4 v[140:143], v2, s[16:19], s57 offen nt\n"
      ".Lfd_u7:\n"
      "s_memrealtime vcc\n"
      "s_waitcnt lgkmcnt(0)\n"
      "s_sub_u32 vcc_lo, vcc_lo, s56\n"
      "s_cmpk_lt_i32 vcc_lo, 98\n"
      "s_cbranch_scc1 .Lfd_u7\n"
      "buffer_load_dwordx4 v[48:51], v4, s[36:39], s58 offen nt\n"
      "s_addk_i32 s58, 0x1000\n"
      "s_and_b32 s58, s58, s59\n"
      ".Lfd_u8:\n"
      "s_memrealtime vcc\n"
      "s_waitcnt lgkmcnt(0)\n"
      "s_sub_u32 vcc_lo, vcc_lo, s56\n"
      "s_cmpk_lt_i32 vcc_lo, 112\n"
      "s_cbranch_scc1 .Lfd_u8\n"
      "s_waitcnt vmcnt(9)\n"
      "buffer_load_dwordx4 v[52:55], v4, s[36:39], s58 offen nt\n"
      "s_addk_i32 s58, 0x1000\n"
      "s_and_b32 s58, s58, s59\n"
      ".Lfd_u9:\n"
      "s_memrealtime vcc\n"
      "s_waitcnt lgkmcnt(0)\n"
      "s_sub_u32 vcc_lo, vcc_lo, s56\n"
      "s_cmpk_lt_i32 vcc_lo, 126\n"
      "s_cbranch_scc1 .Lfd_u9\n"
      "s_waitcnt vmcnt(9)\n"
      "buffer_load_dwordx4 v[56:59], v4, s[36:39], s58 offen nt\n"
      "s_addk_i32 s58, 0x1000\n"
      "s_and_b32 s58, s58, s59\n"
      ".Lfd_u10:\n"
      "s_memrealtime vcc\n"
      "s_waitcnt lgkmcnt(0)\n"
      "s_sub_u32 vcc_lo, vcc_lo, s56\n"
      "s_cmpk_lt_i32 vcc_lo, 140\n"
      "s_cbranch_scc1 .Lfd_u10\n"
      "s_waitcnt vmcnt(9)\n"
      "buffer_load_dwordx4 v[60:63], v4, s[36:39], s58 offen nt\n"
      "s_addk_i32 s58, 0x1000\n"
      "s_and_b32 s58, s58, s59\n"
      "s_waitcnt vmcnt(8)\n"
      "v_perm_b32 v16, v116, v112, s69\n"
      "v_perm_b32 v20, v116, v112, s70\n"
      "v_perm_b32 v24, v117, v113, s69\n"
      "v_perm_b32 v28, v117, v113, s70\n"
      "v_perm_b32 v32, v118, v114, s69\n"
      "v_perm_b32 v36, v118, v114, s70\n"
      "v_perm_b32 v40, v119, v115, s69\n"
      "v_perm_b32 v44, v119, v115, s70\n"
      "v_perm_b32 v17, v124, v120, s69\n"
      "v_perm_b32 v21, v124, v120, s70\n"
      "v_perm_b32 v25, v125, v121, s69\n"
      "v_perm_b32 v29, v125, v121, s70\n"
      "v_perm_b32 v33, v126, v122, s69\n"
      "v_perm_b32 v37, v126, v122, s70\n"
      "v_perm_b32 v41, v127, v123, s69\n"
      "v_perm_b32 v45, v127, v123, s70\n"
      ".Lfd_u11:\n"
      "s_memrealtime vcc\n"
      "s_waitcnt lgkmcnt(0)\n"
      "s_sub_u32 vcc_lo, vcc_lo, s56\n"
      "s_cmpk_lt_i32 vcc_lo, 154\n"
      "s_cbranch_scc1 .Lfd_u11\n"
      "s_waitcnt vmcnt(9)\n"
      "buffer_load_dwordx4 v[64:67], v4, s[36:39], s58 offen nt\n"
      "s_addk_i32 s58, 0x1000\n"
      "s_and_b32 s58, s58, s59\n"
      "s_waitcnt vmcnt(7)\n"
      "v_perm_b32 v18, v132, v128, s69\n"
      "v_perm_b32 v22, v132, v128, s70\n"
      "v_perm_b32 v26, v133, v129, s69\n"
      "v_perm_b32 v30, v133, v129, s70\n"
      "v_perm_b32 v34, v134, v130, s69\n"
      "v_perm_b32 v38, v134, v130, s70\n"
      "v_perm_b32 v42, v135, v131, s69\n"
      "v_perm_b32 v46, v135, v131, s70\n"
      ".Lfd_u12:\n"
      "s_memrealtime vcc\n"
      "s_waitcnt lgkmcnt(0)\n"
      "s_sub_u32 vcc_lo, vcc_lo, s56\n"
      "s_cmpk_lt_i32 vcc_lo, 168\n"
      "s_cbranch_scc1 .Lfd_u12\n"
      "s_waitcnt vmcnt(9)\n"
      "buffer_load_dwordx4 v[68:71], v4, s[36:39], s58 offen nt\n"
      "s_addk_i32 s58, 0x1000\n"
      "s_and_b32 s58, s58, s59\n"
      "s_waitcnt vmcnt(6)\n"
      "v_perm_b32 v19, v140, v136, s69\n"
      "v_perm_b32 v23, v140, v136, s70\n"
      "v_perm_b32 v27, v141, v137, s69\n"
      "v_perm_b32 v31, v141, v137, s70\n"
      "v_perm_b32 v35, v142, v138, s69\n"
      "v_perm_b32 v39, v142, v138, s70\n"
      "v_perm_b32 v43, v143, v139, s69\n"
      "v_perm_b32 v47, v143, v139, s70\n"
      "v_mfma_f32_16x16x32_bf16 v[148:151], v[144:147], v[16:19], 0\n"
      "v_mfma_f32_16x16x32_bf16 v[152:155], v[144:147], v[20:23], 0\n"
      "v_mfma_f32_16x16x32_bf16 v[156:159], v[144:147], v[24:27], 0\n"
      "v_mfma_f32_16x16x32_bf16 v[160:163], v[144:147], v[28:31], 0\n"
      "v_mfma_f32_16x16x32_bf16 v[164:167], v[144:147], v[32:35], 0\n"
      "v_mfma_f32_16x16x32_bf16 v[168:171], v[144:147], v[36:39], 0\n"
      "v_mfma_f32_16x16x32_bf16 v[172:175], v[144:147], v[40:43], 0\n"
      "v_mfma_f32_16x16x32_bf16 v[176:179], v[144:147], v[44:47], 0\n"
      "s_mov_b32 exec_lo, 0xff0000ff\n"
      "s_mov_b32 exec_hi, 0xff0000ff\n"
      "v_mov_b32 v149, v152\n"
      "v_mov_b32 v150, v156\n"
      "v_mov_b32 v151, v160\n"
      "ds_write_b128 v11, v[148:151] offset:0\n"
      "v_mov_b32 v165, v168\n"
      "v_mov_b32 v166, v172\n"
      "v_mov_b32 v167, v176\n"
      "ds_write_b128 v11, v[164:167] offset:512\n"
      "s_mov_b64 exec, -1\n"
      "s_waitcnt lgkmcnt(0)\n"
      "s_barrier\n"
      "s_waitcnt vmcnt(9)\n"
      "buffer_load_dwordx4 v[72:75], v4, s[36:39], s58 offen nt\n"
      "s_addk_i32 s58, 0x1000\n"
      "s_and_b32 s58, s58, s59\n"
      ".Lfd_u13:\n"
      "s_memrealtime vcc\n"
      "s_waitcnt lgkmcnt(0)\n"
      "s_sub_u32 vcc_lo, vcc_lo, s56\n"
      "s_cmpk_lt_i32 vcc_lo, 196\n"
      "s_cbranch_scc1 .Lfd_u13\n"
      "s_waitcnt vmcnt(9)\n"
      "buffer_load_dwordx4 v[76:79], v4, s[36:39], s58 offen nt\n"
      "s_addk_i32 s58, 0x1000\n"
      "s_and_b32 s58, s58, s59\n"
      ".Lfd_u14:\n"
      "s_memrealtime vcc\n"
      "s_waitcnt lgkmcnt(0)\n"
      "s_sub_u32 vcc_lo, vcc_lo, s56\n"
      "s_cmpk_lt_i32 vcc_lo, 210\n"
      "s_cbranch_scc1 .Lfd_u14\n"
      "s_waitcnt vmcnt(9)\n"
      "buffer_load_dwordx4 v[80:83], v4, s[36:39], s58 offen nt\n"
      "s_addk_i32 s58, 0x1000\n"
      "s_and_b32 s58, s58, s59\n"
      "s_mov_b64 s[40:41], s[24:25]\n"
      "s_mov_b32 s42, s38\n"
      "s_mov_b64 s[44:45], s[26:27]\n"
      "s_mov_b64 s[52:53], s[30:31]\n"
      ".Lfd_u15:\n"
      "s_memrealtime vcc\n"
      "s_waitcnt lgkmcnt(0)\n"
      "s_sub_u32 vcc_lo, vcc_lo, s56\n"
      "s_cmpk_lt_i32 vcc_lo, 224\n"
      "s_cbranch_scc1 .Lfd_u15\n"
      "s_waitcnt vmcnt(9)\n"
      "buffer_load_dwordx4 v[84:87], v4, s[36:39], s58 offen nt\n"
      "s_addk_i32 s58, 0x1000\n"
      "s_and_b32 s58, s58, s59\n"
      ".Lfd_u16:\n"
      "s_memrealtime vcc\n"
      "s_waitcnt lgkmcnt(0)\n"
      "s_sub_u32 vcc_lo, vcc_lo, s56\n"
      "s_cmpk_lt_i32 vcc_lo, 238\n"
      "s_cbranch_scc1 .Lfd_u16\n"
      "s_waitcnt vmcnt(9)\n"
      "buffer_load_dwordx4 v[88:91], v4, s[36:39], s58 offen nt\n"
      "s_addk_i32 s58, 0x1000\n"
      "s_and_b32 s58, s58, s59\n"
      ".Lfd_u17:\n"
      "s_memrealtime vcc\n"
      "s_waitcnt lgkmcnt(0)\n"
      "s_sub_u32 vcc_lo, vcc_lo, s56\n"
      "s_cmpk_lt_i32 vcc_lo, 252\n"
      "s_cbranch_scc1 .Lfd_u17\n"
      "s_waitcnt vmcnt(9)\n"
      "buffer_load_dwordx4 v[92:95], v4, s[36:39], s58 offen nt\n"
      "s_addk_i32 s58, 0x1000\n"
      "s_and_b32 s58, s58, s59\n"
      ".Lfd_u18:\n"
      "s_memrealtime vcc\n"
      "s_waitcnt lgkmcnt(0)\n"
      "s_sub_u32 vcc_lo, vcc_lo, s56\n"
      "s_cmpk_lt_i32 vcc_lo, 266\n"
      "s_cbranch_scc1 .Lfd_u18\n"
      "s_waitcnt vmcnt(9)\n"
      "buffer_load_dwordx4 v[96:99], v4, s[36:39], s58 offen nt\n"
      "s_addk_i32 s58, 0x1000\n"
      "s_and_b32 s58, s58, s59\n"
      ".Lfd_u19:\n"
      "s_memrealtime vcc\n"
      "s_waitcnt lgkmcnt(0)\n"
      "s_sub_u32 vcc_lo, vcc_lo, s56\n"
      "s_cmpk_lt_i32 vcc_lo, 280\n"
      "s_cbranch_scc1 .Lfd_u19\n"
      "s_waitcnt vmcnt(9)\n"
      "buffer_load_dwordx4 v[100:103], v4, s[36:39], s58 offen nt\n"
      "s_addk_i32 s58, 0x1000\n"
      "s_and_b32 s58, s58, s59\n"
      ".Lfd_u20:\n"
      "s_memrealtime vcc\n"
      "s_waitcnt lgkmcnt(0)\n"
      "s_sub_u32 vcc_lo, vcc_lo, s56\n"
      "s_cmpk_lt_i32 vcc_lo, 294\n"
      "s_cbranch_scc1 .Lfd_u20\n"
      "s_waitcnt vmcnt(9)\n"
      "buffer_load_dwordx4 v[104:107], v4, s[36:39], s58 offen nt\n"
      "s_addk_i32 s58, 0x1000\n"
      "s_and_b32 s58, s58, s59\n"
      ".Lfd_u21:\n"
      "s_memrealtime vcc\n"
      "s_waitcnt lgkmcnt(0)\n"
      "s_sub_u32 vcc_lo, vcc_lo, s56\n"
      "s_cmpk_lt_i32 vcc_lo, 308\n"
      "s_cbranch_scc1 .Lfd_u21\n"
      "s_waitcnt vmcnt(9)\n"
      "buffer_load_dwordx4 v[108:111], v4, s[36:39], s58 offen nt\n"
      "s_addk_i32 s58, 0x1000\n"
      "s_and_b32 s58, s58, s59\n"
      ".Lfd_u22:\n"
      "s_memrealtime vcc\n"
      "s_waitcnt lgkmcnt(0)\n"
      "s_sub_u32 vcc_lo, vcc_lo, s56\n"
      "s_cmpk_lt_i32 vcc_lo, 322\n"
      "s_cbranch_scc1 .Lfd_u22\n"
      "s_waitcnt vmcnt(9)\n"
      "buffer_load_dwordx4 v[112:115], v5, s[40:43], s58 offen nt\n"
      "s_addk_i32 s58, 0x1000\n"
      "s_and_b32 s58, s58, s59\n"
      ".Lfd_u23:\n"
      "s_memrealtime vcc\n"
      "s_waitcnt lgkmcnt(0)\n"
      "s_sub_u32 vcc_lo, vcc_lo, s56\n"
      "s_cmpk_lt_i32 vcc_lo, 336\n"
      "s_cbranch_scc1 .Lfd_u23\n"
      "s_waitcnt vmcnt(9)\n"
      "buffer_load_dwordx4 v[116:119], v5, s[40:43], s58 offen nt\n"
      "s_addk_i32 s58, 0x1000\n"
      "s_and_b32 s58, s58, s59\n"
      ".Lfd_u24:\n"
      "s_memrealtime vcc\n"
      "s_waitcnt lgkmcnt(0)\n"
      "s_sub_u32 vcc_lo, vcc_lo, s56\n"
      "s_cmpk_lt_i32 vcc_lo, 350\n"
      "s_cbranch_scc1 .Lfd_u24\n"
      "s_waitcnt vmcnt(9)\n"
      "buffer_load_dwordx4 v[120:123], v5, s[40:43], s58 offen nt\n"
      "s_addk_i32 s58, 0x1000\n"
      "s_and_b32 s58, s58, s59\n"
      ".Lfd_u25:\n"
      "s_memrealtime vcc\n"
      "s_waitcnt lgkmcnt(0)\n"
      "s_sub_u32 vcc_lo, vcc_lo, s56\n"
      "s_cmpk_lt_i32 vcc_lo, 364\n"
      "s_cbranch_scc1 .Lfd_u25\n"
      "s_waitcnt vmcnt(9)\n"
      "buffer_load_dwordx4 v[124:127], v5, s[40:43], s58 offen nt\n"
      "s_addk_i32 s58, 0x1000\n"
      "s_and_b32 s58, s58, s59\n"
      ".Lfd_u26:\n"
      "s_memrealtime vcc\n"
      "s_waitcnt lgkmcnt(0)\n"
      "s_sub_u32 vcc_lo, vcc_lo, s56\n"
      "s_cmpk_lt_i32 vcc_lo, 378\n"
      "s_cbranch_scc1 .Lfd_u26\n"
      "s_waitcnt vmcnt(9)\n"
      "buffer_load_dwordx4 v[128:131], v5, s[40:43], s58 offen nt\n"
      "s_addk_i32 s58, 0x1000\n"
      "s_and_b32 s58, s58, s59\n"
      ".Lfd_u27:\n"
      "s_memrealtime vcc\n"
      "s_waitcnt lgkmcnt(0)\n"
      "s_sub_u32 vcc_lo, vcc_lo, s56\n"
      "s_cmpk_lt_i32 vcc_lo, 392\n"
      "s_cbranch_scc1 .Lfd_u27\n"
      "s_waitcnt vmcnt(9)\n"
      "buffer_load_dwordx4 v[132:135], v5, s[40:43], s58 offen nt\n"
      "s_addk_i32 s58, 0x1000\n"
      "s_and_b32 s58, s58, s59\n"
      ".Lfd_u28:\n"
      "s_memrealtime vcc\n"
      "s_waitcnt lgkmcnt(0)\n"
      "s_sub_u32 vcc_lo, vcc_lo, s56\n"
      "s_cmpk_lt_i32 vcc_lo, 406\n"
      "s_cbranch_scc1 .Lfd_u28\n"
      "s_waitcnt vmcnt(9)\n"
      "buffer_load_dwordx4 v[136:139], v5, s[40:43], s58 offen nt\n"
      "s_addk_i32 s58, 0x1000\n"
      "s_and_b32 s58, s58, s59\n"
      ".Lfd_u29:\n"
      "s_memrealtime vcc\n"
      "s_waitcnt lgkmcnt(0)\n"
      "s_sub_u32 vcc_lo, vcc_lo, s56\n"
      "s_cmpk_lt_i32 vcc_lo, 420\n"
      "s_cbranch_scc1 .Lfd_u29\n"
      "s_waitcnt vmcnt(9)\n"
      "buffer_load_dwordx4 v[140:143], v5, s[40:43], s58 offen nt\n"
      "s_addk_i32 s58, 0x1000\n"
      "s_and_b32 s58, s58, s59\n"
      ".Lfd_u30:\n"
      "s_memrealtime vcc\n"
      "s_waitcnt lgkmcnt(0)\n"
      "s_sub_u32 vcc_lo, vcc_lo, s56\n"
      "s_cmpk_lt_i32 vcc_lo, 434\n"
      "s_cbranch_scc1 .Lfd_u30\n"
      "s_waitcnt vmcnt(9)\n"
      "buffer_load_dwordx4 v[144:147], v5, s[40:43], s58 offen nt\n"
      "s_addk_i32 s58, 0x1000\n"
      "s_and_b32 s58, s58, s59\n"
      ".Lfd_u31:\n"
      "s_memrealtime vcc\n"
      "s_waitcnt lgkmcnt(0)\n"
      "s_sub_u32 vcc_lo, vcc_lo, s56\n"
      "s_cmpk_lt_i32 vcc_lo, 448\n"
      "s_cbranch_scc1 .Lfd_u31\n"
      "s_waitcnt vmcnt(9)\n"
      "buffer_load_dwordx4 v[148:151], v5, s[40:43], s58 offen nt\n"
      "s_addk_i32 s58, 0x1000\n"
      "s_and_b32 s58, s58, s59\n"
      ".Lfd_u32:\n"
      "s_memrealtime vcc\n"
      "s_waitcnt lgkmcnt(0)\n"
      "s_sub_u32 vcc_lo, vcc_lo, s56\n"
      "s_cmpk_lt_i32 vcc_lo, 462\n"
      "s_cbranch_scc1 .Lfd_u32\n"
      "s_waitcnt vmcnt(9)\n"
      "buffer_load_dwordx4 v[152:155], v5, s[40:43], s58 offen nt\n"
      "s_addk_i32 s58, 0x1000\n"
      "s_and_b32 s58, s58, s59\n"
      "s_waitcnt lgkmcnt(0)\n"
      "s_barrier\n"
      "ds_read_b128 v[184:187], v1 offset:4096\n"
      "ds_read_b64 v[198:199], v15 offset:5120\n"
      "s_waitcnt vmcnt(11)\n"
      "s_waitcnt lgkmcnt(0)\n"
      "v_mfma_f32_16x16x32_bf16 v[48:51], v[48:51], v[184:187], 0\n"
      "v_mfma_f32_16x16x32_bf16 v[52:55], v[52:55], v[184:187], 0\n"
      "v_mfma_f32_16x16x32_bf16 v[56:59], v[56:59], v[184:187], 0\n"
      "v_mfma_f32_16x16x32_bf16 v[60:63], v[60:63], v[184:187], 0\n"
      "v_mfma_f32_16x16x32_bf16 v[64:67], v[64:67], v[184:187], 0\n"
      "v_mfma_f32_16x16x32_bf16 v[68:71], v[68:71], v[184:187], 0\n"
      "v_mfma_f32_16x16x32_bf16 v[72:75], v[72:75], v[184:187], 0\n"
      "v_mfma_f32_16x16x32_bf16 v[76:79], v[76:79], v[184:187], 0\n"
      "v_mfma_f32_16x16x32_bf16 v[80:83], v[80:83], v[184:187], 0\n"
      "v_mfma_f32_16x16x32_bf16 v[84:87], v[84:87], v[184:187], 0\n"
      "v_mfma_f32_16x16x32_bf16 v[88:91], v[88:91], v[184:187], 0\n"
      "v_mfma_f32_16x16x32_bf16 v[92:95], v[92:95], v[184:187], 0\n"
      "v_mfma_f32_16x16x32_bf16 v[96:99], v[96:99], v[184:187], 0\n"
      "v_mfma_f32_16x16x32_bf16 v[100:103], v[100:103], v[184:187], 0\n"
      "s_waitcnt vmcnt(9)\n"
      "buffer_load_dwordx4 v[156:159], v5, s[40:43], s58 offen nt\n"
      "s_addk_i32 s58, 0x1000\n"
      "s_and_b32 s58, s58, s59\n"
      "v_mfma_f32_16x16x32_bf16 v[104:107], v[104:107], v[184:187], 0\n"
      "v_mfma_f32_16x16x32_bf16 v[108:111], v[108:111], v[184:187], 0\n"
      "v_readfirstlane_b32 s73, v198\n"
      "v_readfirstlane_b32 s75, v199\n"
      "v_add_f32_dpp v48, v49, v48 quad_perm:[1,1,1,1] row_mask:0xf bank_mask:0xf bound_ctrl:1\n"
      "v_add_f32_dpp v52, v53, v52 quad_perm:[1,1,1,1] row_mask:0xf bank_mask:0xf bound_ctrl:1\n"
      "v_add_f32_dpp v56, v57, v56 quad_perm:[1,1,1,1] row_mask:0xf bank_mask:0xf bound_ctrl:1\n"
      "v_add_f32_dpp v60, v61, v60 quad_perm:[1,1,1,1] row_mask:0xf bank_mask:0xf bound_ctrl:1\n"
      "v_add_f32_dpp v64, v65, v64 quad_perm:[1,1,1,1] row_mask:0xf bank_mask:0xf bound_ctrl:1\n"
      "v_add_f32_dpp v68, v69, v68 quad_perm:[1,1,1,1] row_mask:0xf bank_mask:0xf bound_ctrl:1\n"
      "v_add_f32_dpp v72, v73, v72 quad_perm:[1,1,1,1] row_mask:0xf bank_mask:0xf bound_ctrl:1\n"
      "v_add_f32_dpp v76, v77, v76 quad_perm:[1,1,1,1] row_mask:0xf bank_mask:0xf bound_ctrl:1\n"
      "v_add_f32_dpp v80, v81, v80 quad_perm:[1,1,1,1] row_mask:0xf bank_mask:0xf bound_ctrl:1\n"
      "v_add_f32_dpp v84, v85, v84 quad_perm:[1,1,1,1] row_mask:0xf bank_mask:0xf bound_ctrl:1\n"
      "v_add_f32_dpp v88, v89, v88 quad_perm:[1,1,1,1] row_mask:0xf bank_mask:0xf bound_ctrl:1\n"
      "v_add_f32_dpp v92, v93, v92 quad_perm:[1,1,1,1] row_mask:0xf bank_mask:0xf bound_ctrl:1\n"
      "v_add_f32_dpp v96, v97, v96 quad_perm:[1,1,1,1] row_mask:0xf bank_mask:0xf bound_ctrl:1\n"
      "v_add_f32_dpp v100, v101, v100 quad_perm:[1,1,1,1] row_mask:0xf bank_mask:0xf bound_ctrl:1\n"
      "v_add_f32_dpp v104, v105, v104 quad_perm:[1,1,1,1] row_mask:0xf bank_mask:0xf bound_ctrl:1\n"
      "v_add_f32_dpp v108, v109, v108 quad_perm:[1,1,1,1] row_mask:0xf bank_mask:0xf bound_ctrl:1\n"
      "v_add_f32_dpp v48, v50, v48 quad_perm:[2,2,2,2] row_mask:0xf bank_mask:0xf bound_ctrl:1\n"
      "v_add_f32_dpp v52, v54, v52 quad_perm:[2,2,2,2] row_mask:0xf bank_mask:0xf bound_ctrl:1\n"
      "v_add_f32_dpp v56, v58, v56 quad_perm:[2,2,2,2] row_mask:0xf bank_mask:0xf bound_ctrl:1\n"
      "v_add_f32_dpp v60, v62, v60 quad_perm:[2,2,2,2] row_mask:0xf bank_mask:0xf bound_ctrl:1\n"
      "v_add_f32_dpp v64, v66, v64 quad_perm:[2,2,2,2] row_mask:0xf bank_mask:0xf bound_ctrl:1\n"
      "v_add_f32_dpp v68, v70, v68 quad_perm:[2,2,2,2] row_mask:0xf bank_mask:0xf bound_ctrl:1\n"
      "v_add_f32_dpp v72, v74, v72 quad_perm:[2,2,2,2] row_mask:0xf bank_mask:0xf bound_ctrl:1\n"
      "v_add_f32_dpp v76, v78, v76 quad_perm:[2,2,2,2] row_mask:0xf bank_mask:0xf bound_ctrl:1\n"
      "v_add_f32_dpp v80, v82, v80 quad_perm:[2,2,2,2] row_mask:0xf bank_mask:0xf bound_ctrl:1\n"
      "v_add_f32_dpp v84, v86, v84 quad_perm:[2,2,2,2] row_mask:0xf bank_mask:0xf bound_ctrl:1\n"
      "v_add_f32_dpp v88, v90, v88 quad_perm:[2,2,2,2] row_mask:0xf bank_mask:0xf bound_ctrl:1\n"
      "v_add_f32_dpp v92, v94, v92 quad_perm:[2,2,2,2] row_mask:0xf bank_mask:0xf bound_ctrl:1\n"
      "v_add_f32_dpp v96, v98, v96 quad_perm:[2,2,2,2] row_mask:0xf bank_mask:0xf bound_ctrl:1\n"
      "v_add_f32_dpp v100, v102, v100 quad_perm:[2,2,2,2] row_mask:0xf bank_mask:0xf bound_ctrl:1\n"
      "v_add_f32_dpp v104, v106, v104 quad_perm:[2,2,2,2] row_mask:0xf bank_mask:0xf bound_ctrl:1\n"
      "v_add_f32_dpp v108, v110, v108 quad_perm:[2,2,2,2] row_mask:0xf bank_mask:0xf bound_ctrl:1\n"
      "s_waitcnt vmcnt(9)\n"
      "buffer_load_dwordx4 v[160:163], v5, s[40:43], s58 offen nt\n"
      "s_addk_i32 s58, 0x1000\n"
      "s_and_b32 s58, s58, s59\n"
      "v_add_f32_dpp v48, v51, v48 quad_perm:[3,3,3,3] row_mask:0xf bank_mask:0xf bound_ctrl:1\n"
      "v_add_f32_dpp v52, v55, v52 quad_perm:[3,3,3,3] row_mask:0xf bank_mask:0xf bound_ctrl:1\n"
      "v_add_f32_dpp v56, v59, v56 quad_perm:[3,3,3,3] row_mask:0xf bank_mask:0xf bound_ctrl:1\n"
      "v_add_f32_dpp v60, v63, v60 quad_perm:[3,3,3,3] row_mask:0xf bank_mask:0xf bound_ctrl:1\n"
      "v_add_f32_dpp v64, v67, v64 quad_perm:[3,3,3,3] row_mask:0xf bank_mask:0xf bound_ctrl:1\n"
      "v_add_f32_dpp v68, v71, v68 quad_perm:[3,3,3,3] row_mask:0xf bank_mask:0xf bound_ctrl:1\n"
      "v_add_f32_dpp v72, v75, v72 quad_perm:[3,3,3,3] row_mask:0xf bank_mask:0xf bound_ctrl:1\n"
      "v_add_f32_dpp v76, v79, v76 quad_perm:[3,3,3,3] row_mask:0xf bank_mask:0xf bound_ctrl:1\n"
      "v_add_f32_dpp v80, v83, v80 quad_perm:[3,3,3,3] row_mask:0xf bank_mask:0xf bound_ctrl:1\n"
      "v_add_f32_dpp v84, v87, v84 quad_perm:[3,3,3,3] row_mask:0xf bank_mask:0xf bound_ctrl:1\n"
      "v_add_f32_dpp v88, v91, v88 quad_perm:[3,3,3,3] row_mask:0xf bank_mask:0xf bound_ctrl:1\n"
      "v_add_f32_dpp v92, v95, v92 quad_perm:[3,3,3,3] row_mask:0xf bank_mask:0xf bound_ctrl:1\n"
      "v_add_f32_dpp v96, v99, v96 quad_perm:[3,3,3,3] row_mask:0xf bank_mask:0xf bound_ctrl:1\n"
      "v_add_f32_dpp v100, v103, v100 quad_perm:[3,3,3,3] row_mask:0xf bank_mask:0xf bound_ctrl:1\n"
      "v_add_f32_dpp v104, v107, v104 quad_perm:[3,3,3,3] row_mask:0xf bank_mask:0xf bound_ctrl:1\n"
      "v_add_f32_dpp v108, v111, v108 quad_perm:[3,3,3,3] row_mask:0xf bank_mask:0xf bound_ctrl:1\n"
      "v_max3_f32 v196, v48, v52, v56\n"
      "v_max3_f32 v196, v196, v60, v64\n"
      "v_max3_f32 v196, v196, v68, v72\n"
      "v_max3_f32 v196, v196, v76, v80\n"
      "v_max3_f32 v196, v196, v84, v88\n"
      "v_max3_f32 v196, v196, v92, v96\n"
      "v_max3_f32 v196, v196, v100, v104\n"
      "v_max_f32 v196, v196, v108\n"
      "v_mov_b32 v198, v196\n"
      "s_nop 1\n"
      "v_permlane16_swap_b32 v196, v198\n"
      "v_max_f32 v196, v196, v198\n"
      "v_mov_b32 v198, v196\n"
      "s_nop 1\n"
      "v_permlane32_swap_b32 v196, v198\n"
      "v_max_f32 v196, v196, v198\n"
      "v_mul_f32 v196, s71, v196\n"
      "v_fma_f32 v48, v48, s71, -v196\n"
      "v_exp_f32 v48, v48\n"
      "v_fma_f32 v52, v52, s71, -v196\n"
      "v_exp_f32 v52, v52\n"
      "v_fma_f32 v56, v56, s71, -v196\n"
      "v_exp_f32 v56, v56\n"
      "v_fma_f32 v60, v60, s71, -v196\n"
      "v_exp_f32 v60, v60\n"
      "s_waitcnt vmcnt(9)\n"
      "buffer_load_dwordx4 v[164:167], v5, s[40:43], s58 offen nt\n"
      "s_addk_i32 s58, 0x1000\n"
      "s_and_b32 s58, s58, s59\n"
      "v_fma_f32 v64, v64, s71, -v196\n"
      "v_exp_f32 v64, v64\n"
      "v_fma_f32 v68, v68, s71, -v196\n"
      "v_exp_f32 v68, v68\n"
      "v_fma_f32 v72, v72, s71, -v196\n"
      "v_exp_f32 v72, v72\n"
      "v_fma_f32 v76, v76, s71, -v196\n"
      "v_exp_f32 v76, v76\n"
      "v_fma_f32 v80, v80, s71, -v196\n"
      "v_exp_f32 v80, v80\n"
      "v_fma_f32 v84, v84, s71, -v196\n"
      "v_exp_f32 v84, v84\n"
      "v_fma_f32 v88, v88, s71, -v196\n"
      "v_exp_f32 v88, v88\n"
      "v_fma_f32 v92, v92, s71, -v196\n"
      "v_exp_f32 v92, v92\n"
      "v_fma_f32 v96, v96, s71, -v196\n"
      "v_exp_f32 v96, v96\n"
      "v_fma_f32 v100, v100, s71, -v196\n"
      "v_exp_f32 v100, v100\n"
      "v_fma_f32 v104, v104, s71, -v196\n"
      "v_exp_f32 v104, v104\n"
      "v_fma_f32 v108, v108, s71, -v196\n"
      "v_exp_f32 v108, v108\n"
      "v_add_f32 v197, v48, v52\n"
      "v_add_f32 v197, v197, v56\n"
      "v_add_f32 v197, v197, v60\n"
      "v_add_f32 v197, v197, v64\n"
      "v_add_f32 v197, v197, v68\n"
      "v_add_f32 v197, v197, v72\n"
      "v_add_f32 v197, v197, v76\n"
      "v_add_f32 v197, v197, v80\n"
      "v_add_f32 v197, v197, v84\n"
      "v_add_f32 v197, v197, v88\n"
      "v_add_f32 v197, v197, v92\n"
      "v_add_f32 v197, v197, v96\n"
      "v_add_f32 v197, v197, v100\n"
      "v_add_f32 v197, v197, v104\n"
      "v_add_f32 v197, v197, v108\n"
      "s_waitcnt vmcnt(9)\n"
      "buffer_load_dwordx4 v[168:171], v5, s[40:43], s58 offen nt\n"
      "s_addk_i32 s58, 0x1000\n"
      "s_and_b32 s58, s58, s59\n"
      "v_mov_b32 v198, v197\n"
      "s_nop 1\n"
      "v_permlane16_swap_b32 v197, v198\n"
      "v_add_f32 v197, v197, v198\n"
      "v_mov_b32 v198, v197\n"
      "s_nop 1\n"
      "v_permlane32_swap_b32 v197, v198\n"
      "v_add_f32 v197, v197, v198\n"
      "s_mov_b64 exec, 0x1111\n"
      "ds_write_b64 v14, v[196:197] offset:5248\n"
      "s_mov_b64 exec, -1\n"
      "v_cvt_pk_bf16_f32 v176, v48, v52\n"
      "v_cvt_pk_bf16_f32 v177, v56, v60\n"
      "v_cvt_pk_bf16_f32 v178, v64, v68\n"
      "v_cvt_pk_bf16_f32 v179, v72, v76\n"
      "v_cvt_pk_bf16_f32 v180, v80, v84\n"
      "v_cvt_pk_bf16_f32 v181, v88, v92\n"
      "v_cvt_pk_bf16_f32 v182, v96, v100\n"
      "v_cvt_pk_bf16_f32 v183, v104, v108\n"
      "s_mov_b32 vcc_lo, 0xeeeeeeee\n"
      "s_mov_b32 vcc_hi, vcc_lo\n"
      "v_cndmask_b32_dpp v80, v176, v15, vcc quad_perm:[0,0,0,0] row_mask:0xf bank_mask:0xf bound_ctrl:1\n"
      "v_cndmask_b32_dpp v81, v177, v15, vcc quad_perm:[0,0,0,0] row_mask:0xf bank_mask:0xf bound_ctrl:1\n"
      "v_cndmask_b32_dpp v82, v178, v15, vcc quad_perm:[0,0,0,0] row_mask:0xf bank_mask:0xf bound_ctrl:1\n"
      "v_cndmask_b32_dpp v83, v179, v15, vcc quad_perm:[0,0,0,0] row_mask:0xf bank_mask:0xf bound_ctrl:1\n"
      "v_cndmask_b32_dpp v96, v180, v15, vcc quad_perm:[0,0,0,0] row_mask:0xf bank_mask:0xf bound_ctrl:1\n"
      "v_cndmask_b32_dpp v97, v181, v15, vcc quad_perm:[0,0,0,0] row_mask:0xf bank_mask:0xf bound_ctrl:1\n"
      "v_cndmask_b32_dpp v98, v182, v15, vcc quad_perm:[0,0,0,0] row_mask:0xf bank_mask:0xf bound_ctrl:1\n"
      "v_cndmask_b32_dpp v99, v183, v15, vcc quad_perm:[0,0,0,0] row_mask:0xf bank_mask:0xf bound_ctrl:1\n"
      "s_waitcnt lgkmcnt(0)\n"
      "s_barrier\n"
      "s_waitcnt vmcnt(9)\n"
      "buffer_load_dwordx4 v[172:175], v5, s[40:43], s58 offen nt\n"
      "s_mov_b32 vcc_lo, 0xdddddddd\n"
      "s_mov_b32 vcc_hi, vcc_lo\n"
      "v_cndmask_b32_dpp v84, v176, v15, vcc quad_perm:[0,0,0,0] row_mask:0xf bank_mask:0xf bound_ctrl:1\n"
      "v_cndmask_b32_dpp v85, v177, v15, vcc quad_perm:[0,0,0,0] row_mask:0xf bank_mask:0xf bound_ctrl:1\n"
      "v_cndmask_b32_dpp v86, v178, v15, vcc quad_perm:[0,0,0,0] row_mask:0xf bank_mask:0xf bound_ctrl:1\n"
      "v_cndmask_b32_dpp v87, v179, v15, vcc quad_perm:[0,0,0,0] row_mask:0xf bank_mask:0xf bound_ctrl:1\n"
      "v_cndmask_b32_dpp v100, v180, v15, vcc quad_perm:[0,0,0,0] row_mask:0xf bank_mask:0xf bound_ctrl:1\n"
      "v_cndmask_b32_dpp v101, v181, v15, vcc quad_perm:[0,0,0,0] row_mask:0xf bank_mask:0xf bound_ctrl:1\n"
      "v_cndmask_b32_dpp v102, v182, v15, vcc quad_perm:[0,0,0,0] row_mask:0xf bank_mask:0xf bound_ctrl:1\n"
      "v_cndmask_b32_dpp v103, v183, v15, vcc quad_perm:[0,0,0,0] row_mask:0xf bank_mask:0xf bound_ctrl:1\n"
      "s_mov_b32 vcc_lo, 0xbbbbbbbb\n"
      "s_mov_b32 vcc_hi, vcc_lo\n"
      "v_cndmask_b32_dpp v88, v176, v15, vcc quad_perm:[0,0,0,0] row_mask:0xf bank_mask:0xf bound_ctrl:1\n"
      "v_cndmask_b32_dpp v89, v177, v15, vcc quad_perm:[0,0,0,0] row_mask:0xf bank_mask:0xf bound_ctrl:1\n"
      "v_cndmask_b32_dpp v90, v178, v15, vcc quad_perm:[0,0,0,0] row_mask:0xf bank_mask:0xf bound_ctrl:1\n"
      "v_cndmask_b32_dpp v91, v179, v15, vcc quad_perm:[0,0,0,0] row_mask:0xf bank_mask:0xf bound_ctrl:1\n"
      "v_cndmask_b32_dpp v104, v180, v15, vcc quad_perm:[0,0,0,0] row_mask:0xf bank_mask:0xf bound_ctrl:1\n"
      "v_cndmask_b32_dpp v105, v181, v15, vcc quad_perm:[0,0,0,0] row_mask:0xf bank_mask:0xf bound_ctrl:1\n"
      "v_cndmask_b32_dpp v106, v182, v15, vcc quad_perm:[0,0,0,0] row_mask:0xf bank_mask:0xf bound_ctrl:1\n"
      "v_cndmask_b32_dpp v107, v183, v15, vcc quad_perm:[0,0,0,0] row_mask:0xf bank_mask:0xf bound_ctrl:1\n"
      "s_mov_b32 vcc_lo, 0x77777777\n"
      "s_mov_b32 vcc_hi, vcc_lo\n"
      "v_cndmask_b32_dpp v92, v176, v15, vcc quad_perm:[0,0,0,0] row_mask:0xf bank_mask:0xf bound_ctrl:1\n"
      "v_cndmask_b32_dpp v93, v177, v15, vcc quad_perm:[0,0,0,0] row_mask:0xf bank_mask:0xf bound_ctrl:1\n"
      "v_cndmask_b32_dpp v94, v178, v15, vcc quad_perm:[0,0,0,0] row_mask:0xf bank_mask:0xf bound_ctrl:1\n"
      "v_cndmask_b32_dpp v95, v179, v15, vcc quad_perm:[0,0,0,0] row_mask:0xf bank_mask:0xf bound_ctrl:1\n"
      "v_cndmask_b32_dpp v108, v180, v15, vcc quad_perm:[0,0,0,0] row_mask:0xf bank_mask:0xf bound_ctrl:1\n"
      "v_cndmask_b32_dpp v109, v181, v15, vcc quad_perm:[0,0,0,0] row_mask:0xf bank_mask:0xf bound_ctrl:1\n"
      "v_cndmask_b32_dpp v110, v182, v15, vcc quad_perm:[0,0,0,0] row_mask:0xf bank_mask:0xf bound_ctrl:1\n"
      "v_cndmask_b32_dpp v111, v183, v15, vcc quad_perm:[0,0,0,0] row_mask:0xf bank_mask:0xf bound_ctrl:1\n"
      "s_and_b32 s65, s73, 0xff\n"
      "s_bfe_u32 s66, s73, 0x80008\n"
      "s_bfe_u32 s67, s73, 0x80010\n"
      "s_lshr_b32 s68, s73, 24\n"
      "s_add_u32 s61, s61, s75\n"
      "s_add_u32 s62, s62, s75\n"
      "s_add_u32 s63, s63, s75\n"
      "s_add_u32 s76, s63, 0x4000\n"
      "s_add_u32 s77, s63, 0x8000\n"
      "s_add_u32 s78, s63, 0xc000\n"
      "s_waitcnt vmcnt(9)\n"
      "buffer_load_dwordx4 v[48:51], v6, s[44:47], s60 offen nt\n"
      "s_waitcnt vmcnt(9)\n"
      "v_perm_b32 v16, v116, v112, s69\n"
      "v_perm_b32 v20, v116, v112, s70\n"
      "v_perm_b32 v24, v117, v113, s69\n"
      "v_perm_b32 v28, v117, v113, s70\n"
      "v_perm_b32 v32, v118, v114, s69\n"
      "v_perm_b32 v36, v118, v114, s70\n"
      "v_perm_b32 v40, v119, v115, s69\n"
      "v_perm_b32 v44, v119, v115, s70\n"
      "v_perm_b32 v17, v124, v120, s69\n"
      "v_perm_b32 v21, v124, v120, s70\n"
      "v_perm_b32 v25, v125, v121, s69\n"
      "v_perm_b32 v29, v125, v121, s70\n"
      "v_perm_b32 v33, v126, v122, s69\n"
      "v_perm_b32 v37, v126, v122, s70\n"
      "v_perm_b32 v41, v127, v123, s69\n"
      "v_perm_b32 v45, v127, v123, s70\n"
      "v_perm_b32 v18, v132, v128, s69\n"
      "v_perm_b32 v22, v132, v128, s70\n"
      "v_perm_b32 v26, v133, v129, s69\n"
      "v_perm_b32 v30, v133, v129, s70\n"
      "v_perm_b32 v34, v134, v130, s69\n"
      "v_perm_b32 v38, v134, v130, s70\n"
      "v_perm_b32 v42, v135, v131, s69\n"
      "v_perm_b32 v46, v135, v131, s70\n"
      "v_perm_b32 v19, v140, v136, s69\n"
      "v_perm_b32 v23, v140, v136, s70\n"
      "v_perm_b32 v27, v141, v137, s69\n"
      "v_perm_b32 v31, v141, v137, s70\n"
      "v_perm_b32 v35, v142, v138, s69\n"
      "v_perm_b32 v39, v142, v138, s70\n"
      "v_perm_b32 v43, v143, v139, s69\n"
      "v_perm_b32 v47, v143, v139, s70\n"
      "v_mfma_f32_16x16x32_bf16 v[188:191], v[80:83], v[16:19], 0\n"
      "v_mfma_f32_16x16x32_bf16 v[192:195], v[80:83], v[32:35], 0\n"
      "v_mfma_f32_16x16x32_bf16 v[188:191], v[84:87], v[20:23], v[188:191]\n"
      "v_mfma_f32_16x16x32_bf16 v[192:195], v[84:87], v[36:39], v[192:195]\n"
      "v_mfma_f32_16x16x32_bf16 v[188:191], v[88:91], v[24:27], v[188:191]\n"
      "v_mfma_f32_16x16x32_bf16 v[192:195], v[88:91], v[40:43], v[192:195]\n"
      "v_mfma_f32_16x16x32_bf16 v[188:191], v[92:95], v[28:31], v[188:191]\n"
      "v_mfma_f32_16x16x32_bf16 v[192:195], v[92:95], v[44:47], v[192:195]\n"
      "s_waitcnt vmcnt(9)\n"
      "buffer_load_dwordx4 v[52:55], v6, s[44:47], s60 offen offset:1024 nt\n"
      "s_addk_i32 s60, 0x2000\n"
      ".Lfd_u33:\n"
      "s_memrealtime vcc\n"
      "s_waitcnt lgkmcnt(0)\n"
      "s_sub_u32 vcc_lo, vcc_lo, s56\n"
      "s_cmpk_lt_i32 vcc_lo, 574\n"
      "s_cbranch_scc1 .Lfd_u33\n"
      "s_waitcnt vmcnt(9)\n"
      "buffer_load_dwordx4 v[56:59], v6, s[44:47], s60 offen nt\n"
      "s_waitcnt vmcnt(9)\n"
      "v_perm_b32 v16, v148, v144, s69\n"
      "v_perm_b32 v20, v148, v144, s70\n"
      "v_perm_b32 v24, v149, v145, s69\n"
      "v_perm_b32 v28, v149, v145, s70\n"
      "v_perm_b32 v32, v150, v146, s69\n"
      "v_perm_b32 v36, v150, v146, s70\n"
      "v_perm_b32 v40, v151, v147, s69\n"
      "v_perm_b32 v44, v151, v147, s70\n"
      ".Lfd_u34:\n"
      "s_memrealtime vcc\n"
      "s_waitcnt lgkmcnt(0)\n"
      "s_sub_u32 vcc_lo, vcc_lo, s56\n"
      "s_cmpk_lt_i32 vcc_lo, 588\n"
      "s_cbranch_scc1 .Lfd_u34\n"
      "s_waitcnt vmcnt(9)\n"
      "buffer_load_dwordx4 v[60:63], v6, s[44:47], s60 offen offset:1024 nt\n"
      "s_addk_i32 s60, 0x2000\n"
      "buffer_load_dwordx4 v[200:203], v8, s[48:51], s62 offen sc1\n"
      "s_waitcnt vmcnt(9)\n"
      "v_perm_b32 v17, v156, v152, s69\n"
      "v_perm_b32 v21, v156, v152, s70\n"
      "v_perm_b32 v25, v157, v153, s69\n"
      "v_perm_b32 v29, v157, v153, s70\n"
      "v_perm_b32 v33, v158, v154, s69\n"
      "v_perm_b32 v37, v158, v154, s70\n"
      "v_perm_b32 v41, v159, v155, s69\n"
      "v_perm_b32 v45, v159, v155, s70\n"
      "s_waitcnt vmcnt(7)\n"
      "v_perm_b32 v18, v164, v160, s69\n"
      "v_perm_b32 v22, v164, v160, s70\n"
      "v_perm_b32 v26, v165, v161, s69\n"
      "v_perm_b32 v30, v165, v161, s70\n"
      "v_perm_b32 v34, v166, v162, s69\n"
      "v_perm_b32 v38, v166, v162, s70\n"
      "v_perm_b32 v42, v167, v163, s69\n"
      "v_perm_b32 v46, v167, v163, s70\n"
      "s_waitcnt vmcnt(5)\n"
      "v_perm_b32 v19, v172, v168, s69\n"
      "v_perm_b32 v23, v172, v168, s70\n"
      "v_perm_b32 v27, v173, v169, s69\n"
      "v_perm_b32 v31, v173, v169, s70\n"
      "v_perm_b32 v35, v174, v170, s69\n"
      "v_perm_b32 v39, v174, v170, s70\n"
      "v_perm_b32 v43, v175, v171, s69\n"
      "v_perm_b32 v47, v175, v171, s70\n"
      "v_mfma_f32_16x16x32_bf16 v[188:191], v[96:99], v[16:19], v[188:191]\n"
      "v_mfma_f32_16x16x32_bf16 v[192:195], v[96:99], v[32:35], v[192:195]\n"
      "v_mfma_f32_16x16x32_bf16 v[188:191], v[100:103], v[20:23], v[188:191]\n"
      "v_mfma_f32_16x16x32_bf16 v[192:195], v[100:103], v[36:39], v[192:195]\n"
      "v_mfma_f32_16x16x32_bf16 v[188:191], v[104:107], v[24:27], v[188:191]\n"
      "v_mfma_f32_16x16x32_bf16 v[192:195], v[104:107], v[40:43], v[192:195]\n"
      "v_mfma_f32_16x16x32_bf16 v[188:191], v[108:111], v[28:31], v[188:191]\n"
      "v_mfma_f32_16x16x32_bf16 v[192:195], v[108:111], v[44:47], v[192:195]\n"
      "s_nop 6\n"
      "ds_write_b128 v12, v[188:191] offset:5632\n"
      "ds_write_b128 v12, v[192:195] offset:6656\n"
      "s_waitcnt lgkmcnt(0)\n"
      "s_barrier\n"
      "s_waitcnt vmcnt(9)\n"
      "buffer_load_dwordx4 v[64:67], v6, s[44:47], s60 offen nt\n"
      ".Lfd_u35:\n"
      "s_memrealtime vcc\n"
      "s_waitcnt lgkmcnt(0)\n"
      "s_sub_u32 vcc_lo, vcc_lo, s56\n"
      "s_cmpk_lt_i32 vcc_lo, 616\n"
      "s_cbranch_scc1 .Lfd_u35\n"
      "s_waitcnt vmcnt(9)\n"
      "buffer_load_dwordx4 v[68:71], v6, s[44:47], s60 offen offset:1024 nt\n"
      "s_addk_i32 s60, 0x2000\n"
      ".Lfd_u36:\n"
      "s_memrealtime vcc\n"
      "s_waitcnt lgkmcnt(0)\n"
      "s_sub_u32 vcc_lo, vcc_lo, s56\n"
      "s_cmpk_lt_i32 vcc_lo, 630\n"
      "s_cbranch_scc1 .Lfd_u36\n"
      "s_waitcnt vmcnt(9)\n"
      "buffer_load_dwordx4 v[72:75], v6, s[44:47], s60 offen nt\n"
      ".Lfd_u37:\n"
      "s_memrealtime vcc\n"
      "s_waitcnt lgkmcnt(0)\n"
      "s_sub_u32 vcc_lo, vcc_lo, s56\n"
      "s_cmpk_lt_i32 vcc_lo, 644\n"
      "s_cbranch_scc1 .Lfd_u37\n"
      "s_waitcnt vmcnt(9)\n"
      "buffer_load_dwordx4 v[76:79], v6, s[44:47], s60 offen offset:1024 nt\n"
      "s_waitcnt vmcnt(4)\n"
      "v_xor_b32 v112, s65, v200\n"
      "v_xor_b32 v113, s66, v201\n"
      "v_xor_b32 v114, s67, v202\n"
      "v_xor_b32 v115, s68, v203\n"
      "v_or3_b32 v116, v112, v113, v114\n"
      "v_bitop3_b32 v116, v116, v115, s79 bitop3:0xa8\n"
      "v_cmp_ne_u32 vcc, 0, v116\n"
      "s_cbranch_vccz .Lfd_ml_ok\n"
      ".Lfd_ml_rty:\n"
      "s_mov_b64 exec, vcc\n"
      "buffer_load_dwordx4 v[200:203], v8, s[48:51], s62 offen sc1\n"
      "s_mov_b64 exec, -1\n"
      "s_addk_i32 s72, -1\n"
      "s_cmp_lt_i32 s72, 0\n"
      "s_cbranch_scc1 .Lfd_ml_ok\n"
      ".Lfd_ml_chk:\n"
      "s_waitcnt vmcnt(0)\n"
      "v_xor_b32 v112, s65, v200\n"
      "v_xor_b32 v113, s66, v201\n"
      "v_xor_b32 v114, s67, v202\n"
      "v_xor_b32 v115, s68, v203\n"
      "v_or3_b32 v116, v112, v113, v114\n"
      "v_bitop3_b32 v116, v116, v115, s79 bitop3:0xa8\n"
      "v_cmp_ne_u32 vcc, 0, v116\n"
      "s_cbranch_vccnz .Lfd_ml_rty\n"
      ".Lfd_ml_ok:\n"
      "v_add_f32 v112, v112, v113\n"
      "s_nop 1\n"
      "v_max_f32_dpp v116, v112, v112 row_ror:8 row_mask:0xf bank_mask:0xf bound_ctrl:1\n"
      "v_mov_b32 v117, v116\n"
      "s_nop 1\n"
      "v_permlane16_swap_b32 v116, v117\n"
      "v_max_f32 v116, v116, v117\n"
      "v_mov_b32 v117, v116\n"
      "s_nop 1\n"
      "v_permlane32_swap_b32 v116, v117\n"
      "v_max_f32 v116, v116, v117\n"
      "v_sub_f32 v112, v112, v116\n"
      "v_exp_f32 v112, v112\n"
      "s_nop 0\n"
      "v_mul_f32 v116, v112, v114\n"
      "s_nop 1\n"
      "v_add_f32_dpp v116, v116, v116 row_ror:8 row_mask:0xf bank_mask:0xf bound_ctrl:1\n"
      "v_mov_b32 v117, v116\n"
      "s_nop 1\n"
      "v_permlane16_swap_b32 v116, v117\n"
      "v_add_f32 v116, v116, v117\n"
      "v_mov_b32 v117, v116\n"
      "s_nop 1\n"
      "v_permlane32_swap_b32 v116, v117\n"
      "v_add_f32 v116, v116, v117\n"
      "v_rcp_f32 v116, v116\n"
      "s_nop 0\n"
      "v_mul_f32 v204, v112, v116\n"
      "v_mov_b32 v205, v204\n"
      "s_nop 1\n"
      "v_permlane32_swap_b32 v204, v205\n"
      "s_waitcnt vmcnt(7)\n"
      "v_perm_b32 v16, v52, v48, s69\n"
      "v_perm_b32 v20, v52, v48, s70\n"
      "v_perm_b32 v24, v53, v49, s69\n"
      "v_perm_b32 v28, v53, v49, s70\n"
      "v_perm_b32 v32, v54, v50, s69\n"
      "v_perm_b32 v36, v54, v50, s70\n"
      "v_perm_b32 v40, v55, v51, s69\n"
      "v_perm_b32 v44, v55, v51, s70\n"
      "s_waitcnt vmcnt(5)\n"
      "v_perm_b32 v17, v60, v56, s69\n"
      "v_perm_b32 v21, v60, v56, s70\n"
      "v_perm_b32 v25, v61, v57, s69\n"
      "v_perm_b32 v29, v61, v57, s70\n"
      "v_perm_b32 v33, v62, v58, s69\n"
      "v_perm_b32 v37, v62, v58, s70\n"
      "v_perm_b32 v41, v63, v59, s69\n"
      "v_perm_b32 v45, v63, v59, s70\n"
      "s_waitcnt vmcnt(2)\n"
      "buffer_load_dwordx4 v[112:115], v7, s[48:51], s61 offen sc1\n"
      "buffer_load_dwordx4 v[116:119], v7, s[48:51], s61 offen offset:1024 sc1\n"
      "s_waitcnt vmcnt(4)\n"
      "v_perm_b32 v18, v68, v64, s69\n"
      "v_perm_b32 v22, v68, v64, s70\n"
      "v_perm_b32 v26, v69, v65, s69\n"
      "v_perm_b32 v30, v69, v65, s70\n"
      "v_perm_b32 v34, v70, v66, s69\n"
      "v_perm_b32 v38, v70, v66, s70\n"
      "v_perm_b32 v42, v71, v67, s69\n"
      "v_perm_b32 v46, v71, v67, s70\n"
      "s_waitcnt vmcnt(2)\n"
      "v_perm_b32 v19, v76, v72, s69\n"
      "v_perm_b32 v23, v76, v72, s70\n"
      "v_perm_b32 v27, v77, v73, s69\n"
      "v_perm_b32 v31, v77, v73, s70\n"
      "v_perm_b32 v35, v78, v74, s69\n"
      "v_perm_b32 v39, v78, v74, s70\n"
      "v_perm_b32 v43, v79, v75, s69\n"
      "v_perm_b32 v47, v79, v75, s70\n"
      "s_branch .Lfd_o_chk\n"
      ".Lfd_o_rty:\n"
      "s_mov_b64 exec, vcc\n"
      "buffer_load_dwordx4 v[112:115], v7, s[48:51], s61 offen sc1\n"
      "buffer_load_dwordx4 v[116:119], v7, s[48:51], s61 offen offset:1024 sc1\n"
      "s_mov_b64 exec, -1\n"
      "s_addk_i32 s72, -1\n"
      "s_cmp_lt_i32 s72, 0\n"
      "s_cbranch_scc1 .Lfd_o_ok\n"
      ".Lfd_o_chk:\n"
      "s_waitcnt vmcnt(1)\n"
      "v_xor_b32 v120, s65, v112\n"
      "v_xor_b32 v121, s66, v113\n"
      "v_xor_b32 v122, s67, v114\n"
      "v_xor_b32 v123, s68, v115\n"
      "v_or3_b32 v128, v120, v121, v122\n"
      "s_waitcnt vmcnt(0)\n"
      "v_xor_b32 v124, s65, v116\n"
      "v_xor_b32 v125, s66, v117\n"
      "v_xor_b32 v126, s67, v118\n"
      "v_xor_b32 v127, s68, v119\n"
      "v_or3_b32 v128, v128, v123, v124\n"
      "v_or3_b32 v128, v128, v125, v126\n"
      "v_bitop3_b32 v128, v128, v127, s79 bitop3:0xa8\n"
      "v_cmp_ne_u32 vcc, 0, v128\n"
      "s_cbranch_vccnz .Lfd_o_rty\n"
      ".Lfd_o_ok:\n"
      "v_permlane32_swap_b32 v120, v124\n"
      "v_permlane32_swap_b32 v121, v125\n"
      "v_permlane32_swap_b32 v122, v126\n"
      "v_permlane32_swap_b32 v123, v127\n"
      "v_pk_mul_f32 v[120:121], v[120:121], v[204:205] op_sel_hi:[1,0]\n"
      "v_pk_mul_f32 v[122:123], v[122:123], v[204:205] op_sel_hi:[1,0]\n"
      "v_pk_fma_f32 v[120:121], v[124:125], v[204:205], v[120:121] op_sel:[0,1,0] op_sel_hi:[1,1,1]\n"
      "v_pk_fma_f32 v[122:123], v[126:127], v[204:205], v[122:123] op_sel:[0,1,0] op_sel_hi:[1,1,1]\n"
      "s_mov_b32 vcc_lo, 0\n"
      "s_mov_b32 vcc_hi, -1\n"
      "v_permlane16_swap_b32 v120, v122\n"
      "v_permlane16_swap_b32 v121, v123\n"
      "v_pk_add_f32 v[120:121], v[120:121], v[122:123]\n"
      "s_nop 1\n"
      "v_add_f32_dpp v120, v120, v120 row_ror:8 row_mask:0xf bank_mask:0xf bound_ctrl:1\n"
      "v_add_f32_dpp v121, v121, v121 row_ror:8 row_mask:0xf bank_mask:0xf bound_ctrl:1\n"
      "v_cvt_pk_bf16_f32 v128, v120, v121\n"
      "s_nop 1\n"
      "v_mov_b32_dpp v129, v128 quad_perm:[1,1,1,1] row_mask:0xf bank_mask:0xf bound_ctrl:1\n"
      "v_mov_b32_dpp v130, v128 quad_perm:[2,2,2,2] row_mask:0xf bank_mask:0xf bound_ctrl:1\n"
      "v_mov_b32_dpp v131, v128 quad_perm:[3,3,3,3] row_mask:0xf bank_mask:0xf bound_ctrl:1\n"
      "s_nop 1\n"
      "v_mfma_f32_16x16x32_bf16 v[132:135], v[128:131], v[16:19], 0\n"
      "v_mfma_f32_16x16x32_bf16 v[148:151], v[128:131], v[32:35], 0\n"
      "v_mfma_f32_16x16x32_bf16 v[136:139], v[128:131], v[20:23], 0\n"
      "v_mfma_f32_16x16x32_bf16 v[152:155], v[128:131], v[36:39], 0\n"
      "v_mfma_f32_16x16x32_bf16 v[140:143], v[128:131], v[24:27], 0\n"
      "v_mfma_f32_16x16x32_bf16 v[156:159], v[128:131], v[40:43], 0\n"
      "v_mfma_f32_16x16x32_bf16 v[144:147], v[128:131], v[28:31], 0\n"
      "v_mfma_f32_16x16x32_bf16 v[160:163], v[128:131], v[44:47], 0\n"
      "s_nop 1\n"
      "v_cndmask_b32 v164, v132, v148, vcc\n"
      "s_nop 0\n"
      "v_cndmask_b32 v165, v136, v152, vcc\n"
      "s_nop 0\n"
      "v_cndmask_b32 v166, v140, v156, vcc\n"
      "s_nop 0\n"
      "v_cndmask_b32 v167, v144, v160, vcc\n"
      "ds_write_b128 v13, v[164:167] offset:13824\n"
      "s_cmp_lg_u32 s74, 0\n"
      "s_waitcnt lgkmcnt(0)\n"
      "s_barrier\n"
      "s_cbranch_scc1 .Lfd_exit\n"
      "buffer_load_dwordx4 v[48:51], v9, s[48:51], s63 offen sc1\n"
      "buffer_load_dwordx4 v[52:55], v9, s[48:51], s76 offen sc1\n"
      "buffer_load_dwordx4 v[56:59], v9, s[48:51], s77 offen sc1\n"
      "buffer_load_dwordx4 v[60:63], v9, s[48:51], s78 offen sc1\n"
      "buffer_load_dwordx4 v[64:67], v9, s[48:51], s63 offen sc1\n"
      "buffer_load_dwordx4 v[68:71], v9, s[48:51], s76 offen sc1\n"
      "buffer_load_dwordx4 v[72:75], v9, s[48:51], s77 offen sc1\n"
      "buffer_load_dwordx4 v[76:79], v9, s[48:51], s78 offen sc1\n"
      ".Lfd_y_top:\n"
      "s_waitcnt vmcnt(7)\n"
      "v_xor_b32 v128, s65, v48\n"
      "v_xor_b32 v129, s66, v49\n"
      "v_xor_b32 v130, s67, v50\n"
      "v_xor_b32 v131, s68, v51\n"
      "v_or3_b32 v144, v128, v129, v130\n"
      "s_waitcnt vmcnt(6)\n"
      "v_xor_b32 v132, s65, v52\n"
      "v_xor_b32 v133, s66, v53\n"
      "v_xor_b32 v134, s67, v54\n"
      "v_xor_b32 v135, s68, v55\n"
      "v_or3_b32 v144, v144, v131, v132\n"
      "v_or3_b32 v144, v144, v133, v134\n"
      "s_waitcnt vmcnt(5)\n"
      "v_xor_b32 v136, s65, v56\n"
      "v_xor_b32 v137, s66, v57\n"
      "v_xor_b32 v138, s67, v58\n"
      "v_xor_b32 v139, s68, v59\n"
      "v_or3_b32 v144, v144, v135, v136\n"
      "v_or3_b32 v144, v144, v137, v138\n"
      "s_waitcnt vmcnt(4)\n"
      "v_xor_b32 v140, s65, v60\n"
      "v_xor_b32 v141, s66, v61\n"
      "v_xor_b32 v142, s67, v62\n"
      "v_xor_b32 v143, s68, v63\n"
      "v_or3_b32 v144, v144, v139, v140\n"
      "v_or3_b32 v144, v144, v141, v142\n"
      "v_bitop3_b32 v144, v144, v143, s79 bitop3:0xa8\n"
      "v_cmp_ne_u32 vcc, 0, v144\n"
      "s_cbranch_vccz .Lfd_y_ok\n"
      "s_mov_b64 exec, vcc\n"
      "buffer_load_dwordx4 v[48:51], v9, s[48:51], s63 offen sc1\n"
      "buffer_load_dwordx4 v[52:55], v9, s[48:51], s76 offen sc1\n"
      "buffer_load_dwordx4 v[56:59], v9, s[48:51], s77 offen sc1\n"
      "buffer_load_dwordx4 v[60:63], v9, s[48:51], s78 offen sc1\n"
      "s_waitcnt vmcnt(7)\n"
      "v_xor_b32 v128, s65, v64\n"
      "v_xor_b32 v129, s66, v65\n"
      "v_xor_b32 v130, s67, v66\n"
      "v_xor_b32 v131, s68, v67\n"
      "v_or3_b32 v144, v128, v129, v130\n"
      "s_waitcnt vmcnt(6)\n"
      "v_xor_b32 v132, s65, v68\n"
      "v_xor_b32 v133, s66, v69\n"
      "v_xor_b32 v134, s67, v70\n"
      "v_xor_b32 v135, s68, v71\n"
      "v_or3_b32 v144, v144, v131, v132\n"
      "v_or3_b32 v144, v144, v133, v134\n"
      "s_waitcnt vmcnt(5)\n"
      "v_xor_b32 v136, s65, v72\n"
      "v_xor_b32 v137, s66, v73\n"
      "v_xor_b32 v138, s67, v74\n"
      "v_xor_b32 v139, s68, v75\n"
      "v_or3_b32 v144, v144, v135, v136\n"
      "v_or3_b32 v144, v144, v137, v138\n"
      "s_waitcnt vmcnt(4)\n"
      "v_xor_b32 v140, s65, v76\n"
      "v_xor_b32 v141, s66, v77\n"
      "v_xor_b32 v142, s67, v78\n"
      "v_xor_b32 v143, s68, v79\n"
      "v_or3_b32 v144, v144, v139, v140\n"
      "v_or3_b32 v144, v144, v141, v142\n"
      "v_bitop3_b32 v144, v144, v143, s79 bitop3:0xa8\n"
      "v_cmp_ne_u32 vcc, 0, v144\n"
      "s_cbranch_vccz .Lfd_y_ok\n"
      "s_mov_b64 exec, vcc\n"
      "buffer_load_dwordx4 v[64:67], v9, s[48:51], s63 offen sc1\n"
      "buffer_load_dwordx4 v[68:71], v9, s[48:51], s76 offen sc1\n"
      "buffer_load_dwordx4 v[72:75], v9, s[48:51], s77 offen sc1\n"
      "buffer_load_dwordx4 v[76:79], v9, s[48:51], s78 offen sc1\n"
      "s_addk_i32 s72, -1\n"
      "s_cmp_lt_i32 s72, 0\n"
      "s_cbranch_scc0 .Lfd_y_top\n"
      ".Lfd_y_ok:\n"
      "s_mov_b64 exec, -1\n"
      "v_pk_add_f32 v[128:129], v[128:129], v[132:133]\n"
      "v_pk_add_f32 v[130:131], v[130:131], v[134:135]\n"
      "v_pk_add_f32 v[136:137], v[136:137], v[140:141]\n"
      "v_pk_add_f32 v[138:139], v[138:139], v[142:143]\n"
      "v_pk_add_f32 v[128:129], v[128:129], v[136:137]\n"
      "v_pk_add_f32 v[130:131], v[130:131], v[138:139]\n"
      "s_or_b32 s4, s13, s14\n"
      "s_nop 0\n"
      "v_permlane32_swap_b32 v128, v130\n"
      "v_permlane32_swap_b32 v129, v131\n"
      "v_pk_add_f32 v[128:129], v[128:129], v[130:131]\n"
      "s_nop 1\n"
      "v_add_f32_dpp v130, v128, v128 row_ror:8 row_mask:0xf bank_mask:0xf bound_ctrl:1\n"
      "v_add_f32_dpp v131, v129, v129 row_ror:8 row_mask:0xf bank_mask:0xf bound_ctrl:1\n"
      "v_add_f32_dpp v128, v128, v128 row_ror:8 row_mask:0xf bank_mask:0xf bound_ctrl:1\n"
      "v_add_f32_dpp v129, v129, v129 row_ror:8 row_mask:0xf bank_mask:0xf bound_ctrl:1\n"
      "s_nop 0\n"
      "v_permlane16_swap_b32 v128, v130\n"
      "v_permlane16_swap_b32 v129, v131\n"
      "v_pk_add_f32 v[128:129], v[128:129], v[130:131]\n"
      "s_mov_b32 exec_lo, 0xff\n"
      "s_mov_b32 exec_hi, 0xff\n"
      "v_cvt_pk_bf16_f32 v128, v128, v129\n"
      "buffer_store_dword v128, v10, s[52:55], s64 offen sc1\n"
      "s_cbranch_scc1 .Lfd_exit\n"
      "s_mov_b64 exec, 1\n"
      "v_mov_b32 v129, s73\n"
      "s_mov_b32 s4, 950272\n"
      "buffer_store_dword v129, off, s[48:51], s4 sc1\n"
      "s_branch .Lfd_exit\n"
      ".Lfd_comm:\n"
      "s_lshr_b32 s4, s6, 4\n"
      "s_add_u32 s4, s4, 1\n"
      "s_xor_b32 s15, s4, s7\n"
      "s_bfe_u32 s4, s6, 0x10003\n"
      "s_lshl1_add_u32 s10, s15, s4\n"
      "s_bfe_u32 s11, s6, 0x20001\n"
      "s_and_b32 s12, s6, 1\n"
      "s_and_b32 s4, s7, 2\n"
      "s_or_b32 s12, s12, s4\n"
      "s_xor_b32 s13, s7, 2\n"
      "s_lshr_b32 s14, s6, 2\n"
      "s_and_b32 s74, s6, 3\n"
      "s_and_b32 s43, s6, 1\n"
      "s_lshl2_add_u32 s15, s43, s7\n"
      "s_lshl_b32 s47, s15, 7\n"
      "s_lshl_b32 s42, s43, 13\n"
      "s_or_b32 s42, s42, s47\n"
      "s_add_u32 s42, s42, 966656\n"
      "s_add_u32 s47, s47, 954368\n"
      "s_mov_b32 s4, 950272\n"
      "s_lshr_b32 s46, s6, 1\n"
      "s_waitcnt lgkmcnt(0)\n"
      "s_add_u32 s48, s28, 0x1fffff\n"
      "s_addc_u32 s49, s29, 0\n"
      "s_and_b32 s48, s48, 0xffe00000\n"
      "s_lshl_b32 s50, s3, 20\n"
      "s_or_b32 s48, s48, s50\n"
      "s_mov_b32 s50, 983040\n"
      "s_mov_b32 s51, s19\n"
      "buffer_load_dword v7, off, s[48:51], s4 sc1\n"
      "s_memrealtime vcc\n"
      "s_cmp_lg_u32 s46, 2\n"
      "s_cbranch_scc1 .Lfd_noprobe\n"
      "s_xor_b32 s15, s42, 0x800\n"
      "s_xor_b32 s4, s42, 0x2000\n"
      "v_and_b32 v1, 7, v0\n"
      "v_lshlrev_b32 v1, 4, v1\n"
      "s_mov_b64 exec, 0xff\n"
      "buffer_store_dwordx4 v[0:3], v1, s[48:51], s42 offen sc1\n"
      "buffer_store_dwordx4 v[0:3], v1, s[48:51], s4 offen sc1\n"
      "s_mov_b64 exec, -1\n"
      "s_load_dword s45, s[48:49], s15\n"
      "s_waitcnt lgkmcnt(0)\n"
      "s_memtime s[36:37]\n"
      "s_waitcnt lgkmcnt(0)\n"
      "s_load_dword s45, s[48:49], s42\n"
      "s_waitcnt lgkmcnt(0)\n"
      "s_memtime s[38:39]\n"
      "s_waitcnt lgkmcnt(0)\n"
      "s_load_dword s45, s[48:49], s4\n"
      "s_waitcnt lgkmcnt(0)\n"
      "s_memtime s[40:41]\n"
      "s_waitcnt lgkmcnt(0)\n"
      "s_add_u32 s44, s40, s36\n"
      "s_sub_u32 s44, s44, s38\n"
      "s_sub_u32 s44, s44, s38\n"
      "s_min_i32 s44, s44, 0x190\n"
      "s_max_i32 s44, s44, 0xfffffe70\n"
      "s_sub_u32 s45, 0, s44\n"
      "s_cmp_eq_u32 s43, 0\n"
      "s_cselect_b32 s44, s45, s44\n"
      ".Lfd_noprobe:\n"
      "v_and_b32 v0, 63, v0\n"
      "v_lshlrev_b32 v1, 4, v0\n"
      "v_and_b32 v3, 0x70, v1\n"
      "v_and_b32 v4, 8, v0\n"
      "v_lshl_or_b32 v2, v4, 6, v3\n"
      "v_lshl_or_b32 v3, v4, 7, v3\n"
      "v_and_b32 v4, 16, v0\n"
      "v_lshl_or_b32 v2, v4, 4, v2\n"
      "v_lshl_or_b32 v3, v4, 8, v3\n"
      "v_and_b32 v4, 32, v0\n"
      "v_lshl_or_b32 v2, v4, 2, v2\n"
      "v_and_b32 v4, 48, v1\n"
      "v_lshrrev_b32 v5, 1, v4\n"
      "v_and_b32 v6, 12, v0\n"
      "v_lshl_or_b32 v4, v6, 6, v4\n"
      "v_and_b32 v6, 48, v0\n"
      "v_lshl_or_b32 v4, v6, 2, v4\n"
      "v_lshrrev_b32 v6, 1, v6\n"
      "s_waitcnt lgkmcnt(0)\n"
      "s_mov_b32 s56, vcc_lo\n"
      "s_xor_b32 s4, s9, 2\n"
      "s_lshl_b32 s57, s4, 14\n"
      "s_lshr_b32 s15, s8, 3\n"
      "s_lshl_b32 s4, s15, 11\n"
      "s_or_b32 s57, s57, s4\n"
      "s_and_b32 s59, s8, 7\n"
      "s_lshl_b32 s4, s59, 7\n"
      "s_or_b32 s57, s57, s4\n"
      "s_lshl_b32 s58, s9, 14\n"
      "s_lshl_b32 s4, s15, 12\n"
      "s_or_b32 s58, s58, s4\n"
      "s_lshl1_add_u32 s59, s59, s15\n"
      "s_lshl_b32 s4, s3, 13\n"
      "s_or_b32 s57, s57, s4\n"
      "s_or_b32 s58, s58, s4\n"
      "s_lshl_b32 s61, s9, 16\n"
      "s_lshr_b32 s4, s59, 2\n"
      "s_lshl_b32 s4, s4, 14\n"
      "s_or_b32 s61, s61, s4\n"
      "s_and_b32 s4, s59, 3\n"
      "s_lshl_b32 s4, s4, 11\n"
      "s_or_b32 s61, s61, s4\n"
      "s_add_u32 s61, s61, 163840\n"
      "s_lshr_b32 s62, s9, 2\n"
      "s_lshl_b32 s62, s62, 14\n"
      "s_and_b32 s4, s9, 3\n"
      "s_lshl_b32 s4, s4, 11\n"
      "s_or_b32 s62, s62, s4\n"
      "s_lshl_b32 s4, s59, 7\n"
      "s_or_b32 s62, s62, s4\n"
      "s_add_u32 s62, s62, 131072\n"
      "s_lshl_b32 s63, s12, 16\n"
      "s_lshr_b32 s4, s10, 1\n"
      "s_lshl_b32 s4, s4, 14\n"
      "s_or_b32 s63, s63, s4\n"
      "s_and_b32 s4, s10, 1\n"
      "s_lshl2_add_u32 s4, s4, s11\n"
      "s_lshl_b32 s4, s4, 10\n"
      "s_or_b32 s63, s63, s4\n"
      "s_add_u32 s63, s63, 688128\n"
      "s_mov_b32 s72, 262144\n"
      "s_movk_i32 s79, 0xff\n"
      "s_mov_b32 s71, 0xffffff00\n"
      "v_mov_b32 v15, 0\n"
      "s_waitcnt vmcnt(0)\n"
      "v_readfirstlane_b32 s4, v7\n"
      "s_nop 0\n"
      "s_add_u32 s73, s4, 0x01010101\n"
      "s_and_b32 s65, s73, 0xff\n"
      "s_bfe_u32 s66, s73, 0x80008\n"
      "s_bfe_u32 s67, s73, 0x80010\n"
      "s_lshr_b32 s68, s73, 24\n"
      "v_mov_b32 v8, s65\n"
      "v_mov_b32 v9, s66\n"
      "v_mov_b32 v10, s67\n"
      "v_mov_b32 v11, s68\n"
      "s_cmp_lg_u32 s46, 2\n"
      "s_cbranch_scc1 .Lfd_novote\n"
      "v_cvt_f32_i32 v16, s44\n"
      "v_and_or_b32 v20, v16, s71, v8\n"
      "v_and_or_b32 v21, v16, s71, v9\n"
      "v_and_or_b32 v22, v16, s71, v10\n"
      "v_and_or_b32 v23, v16, s71, v11\n"
      "s_mov_b64 exec, 0xff\n"
      "buffer_store_dwordx4 v[20:23], v1, s[48:51], s47 offen sc1\n"
      "s_mov_b64 exec, -1\n"
      ".Lfd_novote:\n"
      "s_mov_b32 s47, 954368\n"
      "s_waitcnt lgkmcnt(0)\n"
      "s_barrier\n"
      "ds_read_b128 v[16:19], v2 offset:0\n"
      "ds_read_b128 v[20:23], v2 offset:1024\n"
      "ds_read_b128 v[24:27], v2 offset:2048\n"
      "ds_read_b128 v[28:31], v2 offset:3072\n"
      "s_waitcnt lgkmcnt(2)\n"
      "v_pk_add_f32 v[16:17], v[16:17], v[20:21]\n"
      "v_pk_add_f32 v[18:19], v[18:19], v[22:23]\n"
      "s_waitcnt lgkmcnt(0)\n"
      "v_pk_add_f32 v[24:25], v[24:25], v[28:29]\n"
      "v_pk_add_f32 v[26:27], v[26:27], v[30:31]\n"
      "v_pk_add_f32 v[20:21], v[16:17], v[24:25]\n"
      "v_pk_add_f32 v[22:23], v[18:19], v[26:27]\n"
      "v_pk_add_f32 v[16:17], v[16:17], v[24:25]\n"
      "v_pk_add_f32 v[18:19], v[18:19], v[26:27]\n"
      "s_nop 0\n"
      "v_permlane32_swap_b32 v16, v20\n"
      "v_permlane32_swap_b32 v17, v21\n"
      "v_permlane32_swap_b32 v18, v22\n"
      "v_permlane32_swap_b32 v19, v23\n"
      "v_pk_add_f32 v[16:17], v[16:17], v[20:21]\n"
      "v_pk_add_f32 v[18:19], v[18:19], v[22:23]\n"
      "v_and_or_b32 v16, v16, s71, v8\n"
      "v_and_or_b32 v17, v17, s71, v9\n"
      "v_and_or_b32 v18, v18, s71, v10\n"
      "v_and_or_b32 v19, v19, s71, v11\n"
      "s_mov_b32 exec_hi, 0\n"
      "buffer_store_dwordx4 v[16:19], v3, s[48:51], s57 offen sc1\n"
      "s_mov_b32 exec_hi, -1\n"
      ".Lfd_u38:\n"
      "s_memrealtime vcc\n"
      "s_waitcnt lgkmcnt(0)\n"
      "s_sub_u32 vcc_lo, vcc_lo, s56\n"
      "s_cmpk_lt_i32 vcc_lo, 270\n"
      "s_cbranch_scc1 .Lfd_u38\n"
      "buffer_load_dwordx4 v[20:23], v4, s[48:51], s58 offen sc1\n"
      "buffer_load_dwordx4 v[24:27], v4, s[48:51], s58 offen offset:1024 sc1\n"
      "buffer_load_dwordx4 v[28:31], v4, s[48:51], s58 offen offset:2048 sc1\n"
      "buffer_load_dwordx4 v[32:35], v4, s[48:51], s58 offen offset:3072 sc1\n"
      "buffer_load_dwordx4 v[36:39], v1, s[48:51], s47 offen sc1\n"
      "s_branch .Lfd_q_chk\n"
      ".Lfd_q_rty:\n"
      "s_mov_b64 exec, vcc\n"
      "buffer_load_dwordx4 v[20:23], v4, s[48:51], s58 offen sc1\n"
      "buffer_load_dwordx4 v[24:27], v4, s[48:51], s58 offen offset:1024 sc1\n"
      "buffer_load_dwordx4 v[28:31], v4, s[48:51], s58 offen offset:2048 sc1\n"
      "buffer_load_dwordx4 v[32:35], v4, s[48:51], s58 offen offset:3072 sc1\n"
      "buffer_load_dwordx4 v[36:39], v1, s[48:51], s47 offen sc1\n"
      "s_mov_b64 exec, -1\n"
      "s_addk_i32 s72, -1\n"
      "s_cmp_lt_i32 s72, 0\n"
      "s_cbranch_scc1 .Lfd_q_ok\n"
      ".Lfd_q_chk:\n"
      "s_waitcnt vmcnt(4)\n"
      "v_xor_b32 v40, s65, v20\n"
      "v_xor_b32 v41, s66, v21\n"
      "v_xor_b32 v42, s67, v22\n"
      "v_xor_b32 v43, s68, v23\n"
      "v_or3_b32 v60, v40, v41, v42\n"
      "s_waitcnt vmcnt(3)\n"
      "v_xor_b32 v44, s65, v24\n"
      "v_xor_b32 v45, s66, v25\n"
      "v_xor_b32 v46, s67, v26\n"
      "v_xor_b32 v47, s68, v27\n"
      "v_or3_b32 v60, v60, v43, v44\n"
      "v_or3_b32 v60, v60, v45, v46\n"
      "s_waitcnt vmcnt(2)\n"
      "v_xor_b32 v48, s65, v28\n"
      "v_xor_b32 v49, s66, v29\n"
      "v_xor_b32 v50, s67, v30\n"
      "v_xor_b32 v51, s68, v31\n"
      "v_or3_b32 v60, v60, v47, v48\n"
      "v_or3_b32 v60, v60, v49, v50\n"
      "s_waitcnt vmcnt(1)\n"
      "v_xor_b32 v52, s65, v32\n"
      "v_xor_b32 v53, s66, v33\n"
      "v_xor_b32 v54, s67, v34\n"
      "v_xor_b32 v55, s68, v35\n"
      "v_or3_b32 v60, v60, v51, v52\n"
      "v_or3_b32 v60, v60, v53, v54\n"
      "s_waitcnt vmcnt(0)\n"
      "v_xor_b32 v56, s65, v36\n"
      "v_xor_b32 v57, s66, v37\n"
      "v_xor_b32 v58, s67, v38\n"
      "v_xor_b32 v59, s68, v39\n"
      "v_or3_b32 v60, v60, v55, v56\n"
      "v_or3_b32 v60, v60, v57, v58\n"
      "v_bitop3_b32 v60, v60, v59, s79 bitop3:0xa8\n"
      "v_cmp_ne_u32 vcc, 0, v60\n"
      "s_cbranch_vccnz .Lfd_q_rty\n"
      ".Lfd_q_ok:\n"
      "v_pk_add_f32 v[40:41], v[40:41], v[48:49]\n"
      "v_pk_add_f32 v[42:43], v[42:43], v[50:51]\n"
      "v_pk_add_f32 v[44:45], v[44:45], v[52:53]\n"
      "v_pk_add_f32 v[46:47], v[46:47], v[54:55]\n"
      "v_cvt_pk_bf16_f32 v20, v40, v41\n"
      "v_cvt_pk_bf16_f32 v21, v42, v43\n"
      "v_cvt_pk_bf16_f32 v22, v44, v45\n"
      "v_cvt_pk_bf16_f32 v23, v46, v47\n"
      "ds_write_b128 v1, v[20:23] offset:4096\n"
      "v_permlane32_swap_b32 v56, v57\n"
      "v_add_f32 v56, v56, v57\n"
      "v_mov_b32 v57, v56\n"
      "s_nop 1\n"
      "v_permlane16_swap_b32 v56, v57\n"
      "v_add_f32 v56, v56, v57\n"
      "s_nop 1\n"
      "v_add_f32_dpp v56, v56, v56 row_ror:8 row_mask:0xf bank_mask:0xf bound_ctrl:1\n"
      "s_nop 0\n"
      "v_readfirstlane_b32 s4, v56\n"
      "s_nop 0\n"
      "s_cmp_gt_i32 s4, 0\n"
      "s_cselect_b32 s75, 0x2000, 0\n"
      "v_mov_b32 v16, s73\n"
      "v_mov_b32 v17, s75\n"
      "ds_write_b64 v15, v[16:17] offset:5120\n"
      "s_add_u32 s61, s61, s75\n"
      "s_add_u32 s62, s62, s75\n"
      "s_add_u32 s63, s63, s75\n"
      "s_waitcnt lgkmcnt(0)\n"
      "s_barrier\n"
      "s_waitcnt lgkmcnt(0)\n"
      "s_barrier\n"
      "ds_read_b64 v[16:17], v5 offset:5248\n"
      "ds_read_b64 v[18:19], v5 offset:5280\n"
      "ds_read_b64 v[20:21], v5 offset:5312\n"
      "ds_read_b64 v[22:23], v5 offset:5344\n"
      "ds_read_b32 v12, v6 offset:5248\n"
      "ds_read_b32 v13, v6 offset:5280\n"
      "ds_read_b32 v14, v6 offset:5312\n"
      "ds_read_b32 v15, v6 offset:5344\n"
      "s_waitcnt lgkmcnt(0)\n"
      "v_max3_f32 v24, v16, v18, v20\n"
      "v_max_f32 v24, v24, v22\n"
      "v_sub_f32 v16, v16, v24\n"
      "v_exp_f32 v16, v16\n"
      "v_sub_f32 v18, v18, v24\n"
      "v_exp_f32 v18, v18\n"
      "v_sub_f32 v20, v20, v24\n"
      "v_exp_f32 v20, v20\n"
      "v_sub_f32 v22, v22, v24\n"
      "v_exp_f32 v22, v22\n"
      "v_and_b32 v28, s71, v24\n"
      "v_sub_f32 v29, v24, v28\n"
      "v_mul_f32 v30, v16, v17\n"
      "v_fmac_f32 v30, v18, v19\n"
      "v_fmac_f32 v30, v20, v21\n"
      "v_fmac_f32 v30, v22, v23\n"
      "v_mov_b32 v31, 0\n"
      "v_and_or_b32 v28, v28, s71, v8\n"
      "v_and_or_b32 v29, v29, s71, v9\n"
      "v_and_or_b32 v30, v30, s71, v10\n"
      "v_and_or_b32 v31, v31, s71, v11\n"
      "s_mov_b64 exec, 0xff\n"
      "buffer_store_dwordx4 v[28:31], v1, s[48:51], s62 offen sc1\n"
      "s_mov_b64 exec, -1\n"
      "v_max3_f32 v24, v12, v13, v14\n"
      "v_max_f32 v24, v24, v15\n"
      "v_sub_f32 v12, v12, v24\n"
      "v_exp_f32 v12, v12\n"
      "v_sub_f32 v13, v13, v24\n"
      "v_exp_f32 v13, v13\n"
      "v_sub_f32 v14, v14, v24\n"
      "v_exp_f32 v14, v14\n"
      "v_sub_f32 v15, v15, v24\n"
      "v_exp_f32 v15, v15\n"
      "s_cmp_lg_u32 s74, 0\n"
      "s_cbranch_scc1 .Lfd_nopre\n"
      "s_lshl_b32 s4, s13, 16\n"
      "s_lshl_b32 s15, s14, 7\n"
      "s_or_b32 s4, s4, s15\n"
      "s_add_u32 s4, s4, 688128\n"
      "s_add_u32 s4, s4, s75\n"
      "v_and_b32 v64, 7, v0\n"
      "v_lshlrev_b32 v64, 10, v64\n"
      "v_and_b32 v65, 24, v0\n"
      "v_lshl_or_b32 v64, v65, 11, v64\n"
      "s_mov_b32 exec_hi, 0\n"
      "buffer_load_dword v65, v64, s[48:51], s4 offen sc1\n"
      "s_mov_b32 exec_hi, -1\n"
      ".Lfd_nopre:\n"
      "s_waitcnt lgkmcnt(0)\n"
      "s_barrier\n"
      "ds_read_b128 v[16:19], v1 offset:5632\n"
      "ds_read_b128 v[20:23], v1 offset:7680\n"
      "ds_read_b128 v[24:27], v1 offset:9728\n"
      "ds_read_b128 v[28:31], v1 offset:11776\n"
      "ds_read_b128 v[32:35], v1 offset:6656\n"
      "ds_read_b128 v[36:39], v1 offset:8704\n"
      "ds_read_b128 v[40:43], v1 offset:10752\n"
      "ds_read_b128 v[44:47], v1 offset:12800\n"
      "s_waitcnt lgkmcnt(7)\n"
      "v_pk_mul_f32 v[16:17], v[16:17], v[12:13] op_sel_hi:[1,0]\n"
      "v_pk_mul_f32 v[18:19], v[18:19], v[12:13] op_sel_hi:[1,0]\n"
      "s_waitcnt lgkmcnt(6)\n"
      "v_pk_fma_f32 v[16:17], v[20:21], v[12:13], v[16:17] op_sel:[0,1,0] op_sel_hi:[1,1,1]\n"
      "v_pk_fma_f32 v[18:19], v[22:23], v[12:13], v[18:19] op_sel:[0,1,0] op_sel_hi:[1,1,1]\n"
      "s_waitcnt lgkmcnt(5)\n"
      "v_pk_fma_f32 v[16:17], v[24:25], v[14:15], v[16:17] op_sel_hi:[1,0,1]\n"
      "v_pk_fma_f32 v[18:19], v[26:27], v[14:15], v[18:19] op_sel_hi:[1,0,1]\n"
      "s_waitcnt lgkmcnt(4)\n"
      "v_pk_fma_f32 v[16:17], v[28:29], v[14:15], v[16:17] op_sel:[0,1,0] op_sel_hi:[1,1,1]\n"
      "v_pk_fma_f32 v[18:19], v[30:31], v[14:15], v[18:19] op_sel:[0,1,0] op_sel_hi:[1,1,1]\n"
      "v_and_or_b32 v16, v16, s71, v8\n"
      "v_and_or_b32 v17, v17, s71, v9\n"
      "v_and_or_b32 v18, v18, s71, v10\n"
      "v_and_or_b32 v19, v19, s71, v11\n"
      "buffer_store_dwordx4 v[16:19], v1, s[48:51], s61 offen sc1\n"
      "s_waitcnt lgkmcnt(3)\n"
      "v_pk_mul_f32 v[32:33], v[32:33], v[12:13] op_sel_hi:[1,0]\n"
      "v_pk_mul_f32 v[34:35], v[34:35], v[12:13] op_sel_hi:[1,0]\n"
      "s_waitcnt lgkmcnt(2)\n"
      "v_pk_fma_f32 v[32:33], v[36:37], v[12:13], v[32:33] op_sel:[0,1,0] op_sel_hi:[1,1,1]\n"
      "v_pk_fma_f32 v[34:35], v[38:39], v[12:13], v[34:35] op_sel:[0,1,0] op_sel_hi:[1,1,1]\n"
      "s_waitcnt lgkmcnt(1)\n"
      "v_pk_fma_f32 v[32:33], v[40:41], v[14:15], v[32:33] op_sel_hi:[1,0,1]\n"
      "v_pk_fma_f32 v[34:35], v[42:43], v[14:15], v[34:35] op_sel_hi:[1,0,1]\n"
      "s_waitcnt lgkmcnt(0)\n"
      "v_pk_fma_f32 v[32:33], v[44:45], v[14:15], v[32:33] op_sel:[0,1,0] op_sel_hi:[1,1,1]\n"
      "v_pk_fma_f32 v[34:35], v[46:47], v[14:15], v[34:35] op_sel:[0,1,0] op_sel_hi:[1,1,1]\n"
      "v_and_or_b32 v32, v32, s71, v8\n"
      "v_and_or_b32 v33, v33, s71, v9\n"
      "v_and_or_b32 v34, v34, s71, v10\n"
      "v_and_or_b32 v35, v35, s71, v11\n"
      "buffer_store_dwordx4 v[32:35], v1, s[48:51], s61 offen offset:1024 sc1\n"
      "s_waitcnt lgkmcnt(0)\n"
      "s_barrier\n"
      "ds_read_b128 v[48:51], v1 offset:13824\n"
      "ds_read_b128 v[52:55], v1 offset:14848\n"
      "ds_read_b128 v[56:59], v1 offset:15872\n"
      "ds_read_b128 v[60:63], v1 offset:16896\n"
      "s_waitcnt lgkmcnt(2)\n"
      "v_pk_add_f32 v[48:49], v[48:49], v[52:53]\n"
      "v_pk_add_f32 v[50:51], v[50:51], v[54:55]\n"
      "s_waitcnt lgkmcnt(1)\n"
      "v_pk_add_f32 v[48:49], v[48:49], v[56:57]\n"
      "v_pk_add_f32 v[50:51], v[50:51], v[58:59]\n"
      "s_waitcnt lgkmcnt(0)\n"
      "v_pk_add_f32 v[48:49], v[48:49], v[60:61]\n"
      "v_pk_add_f32 v[50:51], v[50:51], v[62:63]\n"
      "v_and_or_b32 v48, v48, s71, v8\n"
      "v_and_or_b32 v49, v49, s71, v9\n"
      "v_and_or_b32 v50, v50, s71, v10\n"
      "v_and_or_b32 v51, v51, s71, v11\n"
      "buffer_store_dwordx4 v[48:51], v1, s[48:51], s63 offen sc1\n"
      ".Lfd_exit:\n"
      ".Lfd_end:\n"
      : "+{v0}"(tid)
      : "{s[0:1]}"(ka), "{s2}"(wg), "i"((uint32_t)(uintptr_t)(__attribute__((address_space(3))) unsigned char*)smem)
      : "v1", "v2", "v3", "v4", "v5", "v6", "v7", "v8", "v9", "v10", "v11", "v12", "v13", "v14", "v15", "v16",
        "v17", "v18", "v19", "v20", "v21", "v22", "v23", "v24", "v25", "v26", "v27", "v28", "v29", "v30", "v31", "v32",
        "v33", "v34", "v35", "v36", "v37", "v38", "v39", "v40", "v41", "v42", "v43", "v44", "v45", "v46", "v47", "v48",
        "v49", "v50", "v51", "v52", "v53", "v54", "v55", "v56", "v57", "v58", "v59", "v60", "v61", "v62", "v63", "v64",
        "v65", "v66", "v67", "v68", "v69", "v70", "v71", "v72", "v73", "v74", "v75", "v76", "v77", "v78", "v79", "v80",
        "v81", "v82", "v83", "v84", "v85", "v86", "v87", "v88", "v89", "v90", "v91", "v92", "v93", "v94", "v95", "v96",
        "v97", "v98", "v99", "v100", "v101", "v102", "v103", "v104", "v105", "v106", "v107", "v108", "v109", "v110", "v111", "v112",
        "v113", "v114", "v115", "v116", "v117", "v118", "v119", "v120", "v121", "v122", "v123", "v124", "v125", "v126", "v127", "v128",
        "v129", "v130", "v131", "v132", "v133", "v134", "v135", "v136", "v137", "v138", "v139", "v140", "v141", "v142", "v143", "v144",
        "v145", "v146", "v147", "v148", "v149", "v150", "v151", "v152", "v153", "v154", "v155", "v156", "v157", "v158", "v159", "v160",
        "v161", "v162", "v163", "v164", "v165", "v166", "v167", "v168", "v169", "v170", "v171", "v172", "v173", "v174", "v175", "v176",
        "v177", "v178", "v179", "v180", "v181", "v182", "v183", "v184", "v185", "v186", "v187", "v188", "v189", "v190", "v191", "v192",
        "v193", "v194", "v195", "v196", "v197", "v198", "v199", "v200", "v201", "v202", "v203", "v204", "v205", "s3", "s4", "s5",
        "s6", "s7", "s8", "s9", "s10", "s11", "s12", "s13", "s14", "s15", "s16", "s17", "s18", "s19", "s20", "s21",
        "s22", "s23", "s24", "s25", "s26", "s27", "s28", "s29", "s30", "s31", "s33", "s34", "s35", "s36", "s37", "s38",
        "s39", "s40", "s41", "s42", "s43", "s44", "s45", "s46", "s47", "s48", "s49", "s50", "s51", "s52", "s53", "s54",
        "s55", "s56", "s57", "s58", "s59", "s60", "s61", "s62", "s63", "s64", "s65", "s66", "s67", "s68", "s69", "s70",
        "s71", "s72", "s73", "s74", "s75", "s76", "s77", "s78", "s79", "vcc", "scc", "memory");
}

extern "C" void kernel_launch(void* const* d_in, const int* in_sizes, int n_in, void* d_out, int out_size, void* d_ws, size_t ws_size,
                              hipStream_t stream) {
  Params p;
  for (int i = 0; i < 2; ++i) {
    p.br[i].x = d_in[5 * i + 0]; p.br[i].k = d_in[5 * i + 1]; p.br[i].v = d_in[5 * i + 2];
    p.br[i].wq = d_in[5 * i + 3]; p.br[i].wo = d_in[5 * i + 4];
    p.br[i].wq_bytes = 512 * 4096 * 2; p.br[i].cfg = 0x00020000u;
    p.br[i].ws = d_ws; p.br[i].out = d_out;
  }
  fused_decode<<<256, 320, 0, stream>>>(p);
}
```
